```python
import torch

def _softmax(x, axis=-1):
    return torch.softmax(x, dim=axis)
def _gelu(x):
    return torch.nn.functional.gelu(x, approximate="tanh")
def _silu(x):
    return torch.nn.functional.silu(x)
def _sigmoid(x):
    return torch.sigmoid(x)
def _layernorm(x, axis=-1, eps=1e-5):
    m = x.mean(dim=axis, keepdim=True)
    v = x.var(dim=axis, keepdim=True, unbiased=False)
    return (x - m) / torch.sqrt(v + eps)
def _scatter_add(tgt, idx, src, axis=0):
    return tgt.clone().index_add_(axis, idx, src)
def _segment_sum(src, seg, n):
    out = torch.zeros((n,) + src.shape[1:], dtype=src.dtype)
    return out.index_add_(0, seg, src)
def _pad(x, pw):
    flat = tuple(v for pair in reversed(pw) for v in pair)
    return torch.nn.functional.pad(x, flat)

kernel_name = 'sequential_preLN__spectral_gfnet__mixer_s_mlp__prompt'

def setup_inputs():

    _w = (lambda a: (lambda t: t.to(torch.bfloat16) if t.is_floating_point() else t)(torch.as_tensor(a)))
    def _A_setup():
        B, S, D = 1, 2048, 640
        Df = D // 2 + 1
        return {
            "x": _w((lambda *s: torch.randn(*s, dtype=torch.bfloat16) * 0.05)(B, S, D)),
            "w_r": _w((lambda *s: torch.randn(*s, dtype=torch.bfloat16) * 0.4)(S, Df) * 0.02),
            "w_i": _w((lambda *s: torch.randn(*s, dtype=torch.bfloat16) * 0.4)(S, Df) * 0.02),
        }
    _A_in = _A_setup()

    def _B_setup():
        B, S, D, F = 1, 2048, 640, 1728
        return {
            "x":  _w((lambda *s: torch.randn(*s, dtype=torch.bfloat16) * 0.05)(B, S, D)),
            "w1": _w((lambda *s: torch.randn(*s, dtype=torch.bfloat16) * 0.8)(D, D) * (D ** -0.5)),
            "w2": _w((lambda *s: torch.randn(*s, dtype=torch.bfloat16) * 0.8)(D, D) * (D ** -0.5)),
            "w3": _w((lambda *s: torch.randn(*s, dtype=torch.bfloat16) * 0.8)(D, F) * (D ** -0.5)),
            "w4": _w((lambda *s: torch.randn(*s, dtype=torch.bfloat16) * 0.8)(F, D) * (F ** -0.5)),
        }
    _B_in = _B_setup()

    return {
        "A_x": _A_in["x"],
        "A_w_r": _A_in["w_r"],
        "A_w_i": _A_in["w_i"],
        "B_x": _B_in["x"],
        "B_w1": _B_in["w1"],
        "B_w2": _B_in["w2"],
        "B_w3": _B_in["w3"],
        "B_w4": _B_in["w4"],
    }

def reference(A_x, A_w_r, A_w_i, B_x, B_w1, B_w2, B_w3, B_w4):
    def _A_ref(x, w_r, w_i):
        B, S, D = x.shape
        X = (lambda x, axes=(-2,-1): torch.fft.rfft2(x.float(), dim=axes))(x, axes=(-2, -1))
        W = w_r + 1j * w_i
        X = X * W
        y = (lambda x, s, axes=(-2,-1): torch.fft.irfft2(x, s=s, dim=axes).to(torch.bfloat16))(X, (S, D), axes=(-2, -1))
        return y

    def _B_ref(x, w1, w2, w3, w4):
        B, S, D = x.shape
        h = _gelu(_layernorm(x, axis=-1) @ w1)
        c = D // 4
        g0 = _pad(h[:, :, 0*c:1*c], ((0, 0), (1, 0), (0, 0)))[:, :S, :]
        g1 = _pad(h[:, :, 1*c:2*c], ((0, 0), (0, 1), (0, 0)))[:, 1:, :]
        g2 = _pad(h[:, :, 2*c:3*c], ((0, 0), (2, 0), (0, 0)))[:, :S, :]
        g3 = _pad(h[:, :, 3*c:   ], ((0, 0), (0, 2), (0, 0)))[:, 2:, :]
        hs = torch.concatenate([g0, g1, g2, g3], axis=-1) @ w2
        x1 = x + hs
        h2 = _gelu(_layernorm(x1, axis=-1) @ w3) @ w4
        return x1 + h2

    x = A_x
    h = x + _A_ref(_layernorm(x, axis=-1), A_w_r, A_w_i)
    h2 = h + _B_ref(_layernorm(h, axis=-1), B_w1, B_w2, B_w3, B_w4)
    return h2
```

```cpp
#include <hip/hip_runtime.h>
#include <hip/hip_bf16.h>
#include <stdint.h>
typedef unsigned short u16;
constexpr int S=2048, D=640, DF=321, F=1728;
__device__ __forceinline__ float bf2f(u16 b){ return __uint_as_float(((unsigned)b)<<16); }
__device__ __forceinline__ u16 f2bf(float f){ unsigned u=__float_as_uint(f); u += 0x7fffu + ((u>>16)&1u); return (u16)(u>>16); }
__device__ __forceinline__ float rbf(float f){ return bf2f(f2bf(f)); }
__device__ float block_sum(float v, float* sm){
  for(int o=32;o>0;o>>=1) v += __shfl_xor(v,o);
  int w=threadIdx.x>>6; __syncthreads(); if((threadIdx.x&63)==0) sm[w]=v; __syncthreads();
  float r=0; for(int i=0;i<(int)(blockDim.x>>6);++i) r+=sm[i]; return r;
}
__device__ float ln_row(float x, float* sm){
  float mean = block_sum(x, sm)/D;
  float dv = x-mean;
  float var = block_sum(dv*dv, sm)/D;
  float mb = rbf(mean), vb = rbf(var);
  float d = rbf(x-mb);
  float s = rbf(sqrtf(rbf(vb+1e-5f)));
  return rbf(d/s);
}
__global__ void k_ln(const u16* x, float* xn){
  __shared__ float sm[16];
  int s=blockIdx.x, d=threadIdx.x;
  xn[s*D+d]=ln_row(bf2f(x[s*D+d]), sm);
}
__global__ void k_rowdft(const float* xn, float2* X){
  __shared__ float row[D]; __shared__ float2 tw[D];
  int s=blockIdx.x;
  for(int i=threadIdx.x;i<D;i+=blockDim.x){ row[i]=xn[s*D+i]; float sn,cs; sincospif(-2.f*i/D,&sn,&cs); tw[i]=make_float2(cs,sn);}
  __syncthreads();
  int k=threadIdx.x; if(k>=DF) return;
  float re=0,im=0; int idx=0;
  for(int d=0;d<D;++d){ re+=row[d]*tw[idx].x; im+=row[d]*tw[idx].y; idx+=k; if(idx>=D) idx-=D; }
  X[s*DF+k]=make_float2(re,im);
}
__global__ void k_coldft(const float2* in, float2* out, float sign, const u16* wr, const u16* wi, float scale){
  __shared__ float2 tw[S];
  for(int i=threadIdx.x;i<S;i+=blockDim.x){ float sn,cs; sincospif(sign*2.f*i/S,&sn,&cs); tw[i]=make_float2(cs,sn);}
  __syncthreads();
  int u=blockIdx.x; int k=threadIdx.x; if(k>=DF) return;
  float re=0,im=0; int idx=0;
  for(int s=0;s<S;++s){ float2 v=in[s*DF+k]; float2 t=tw[idx]; re+=v.x*t.x-v.y*t.y; im+=v.x*t.y+v.y*t.x; idx=(idx+u)&(S-1);}
  if(wr){ float a=bf2f(wr[u*DF+k]), b=bf2f(wi[u*DF+k]); float r2=re*a-im*b, i2=re*b+im*a; re=r2; im=i2; }
  out[u*DF+k]=make_float2(re*scale,im*scale);
}
__global__ void k_rowidft(const float2* Z, const u16* x, float* h, float* xb, float* a1){
  __shared__ float2 row[DF]; __shared__ float2 tw[D]; __shared__ float sm[16];
  int s=blockIdx.x; int d=threadIdx.x;
  if(d<DF) row[d]=Z[s*DF+d];
  { float sn,cs; sincospif(2.f*d/D,&sn,&cs); tw[d]=make_float2(cs,sn);}
  __syncthreads();
  float acc=0; int idx=d;
  for(int k=1;k<DF-1;++k){ acc+=row[k].x*tw[idx].x-row[k].y*tw[idx].y; idx+=d; if(idx>=D) idx-=D; }
  float y=(row[0].x + ((d&1)?-row[DF-1].x:row[DF-1].x) + 2.f*acc)/D;
  y=rbf(y);
  float hv=rbf(bf2f(x[s*D+d])+y);
  h[s*D+d]=hv;
  float b=ln_row(hv,sm); xb[s*D+d]=b;
  a1[s*D+d]=ln_row(b,sm);
}
__device__ float gelu(float x){ float u=0.7978845608028654f*(x+0.044715f*x*x*x); return 0.5f*x*(1.f+tanhf(u)); }
__global__ void k_gemm(const float* A, const u16* W, float* C, int K, int N, int mode, const float* r1){
  int s=blockIdx.x; extern __shared__ float arow[];
  for(int k=threadIdx.x;k<K;k+=blockDim.x){
    if(mode==1){ int g=k/160; int sh = g==0?1:g==1?-1:g==2?2:-2; int r=s-sh; arow[k]=(r>=0&&r<S)?A[r*K+k]:0.f; }
    else arow[k]=A[s*K+k];
  }
  __syncthreads();
  for(int n=threadIdx.x;n<N;n+=blockDim.x){
    float acc=0; for(int k=0;k<K;++k) acc+=arow[k]*bf2f(W[k*N+n]);
    acc=rbf(acc);
    if(mode==0) acc=rbf(gelu(acc));
    if(mode==1) acc=rbf(r1[s*N+n]+acc);
    C[s*N+n]=acc;
  }
}
__global__ void k_ln2(const float* x, float* o){ __shared__ float sm[16]; int s=blockIdx.x,d=threadIdx.x; o[s*D+d]=ln_row(x[s*D+d],sm); }
__global__ void k_fin(const float* h, const float* x1, const float* h2, u16* out){
  int i=blockIdx.x*blockDim.x+threadIdx.x; out[i]=f2bf(h[i]+rbf(x1[i]+h2[i]));
}
extern "C" void kernel_launch(void* const* d_in, const int* in_sizes, int n_in, void* d_out, int out_size, void* d_ws, size_t ws_size, hipStream_t stream){
  const u16* x=(const u16*)d_in[0]; const u16* wr=(const u16*)d_in[1]; const u16* wi=(const u16*)d_in[2];
  const u16* w1=(const u16*)d_in[4]; const u16* w2=(const u16*)d_in[5]; const u16* w3=(const u16*)d_in[6]; const u16* w4=(const u16*)d_in[7];
  char* ws=(char*)d_ws; size_t MB=1<<20;
  float* xn=(float*)(ws); float2* X=(float2*)(ws+8*MB); float2* Y=(float2*)(ws+16*MB); float2* Z=(float2*)(ws+24*MB);
  float* h=(float*)(ws+32*MB); float* xb=(float*)(ws+40*MB); float* a1=(float*)(ws+48*MB); float* g1=(float*)(ws+56*MB);
  float* x1=(float*)(ws+64*MB); float* a3=(float*)(ws+72*MB); float* g3=(float*)(ws+80*MB); float* h2=(float*)(ws+96*MB);
  k_ln<<<S,D,0,stream>>>(x,xn);
  k_rowdft<<<S,384,0,stream>>>(xn,X);
  k_coldft<<<S,384,0,stream>>>(X,Y,-1.f,wr,wi,1.f);
  k_coldft<<<S,384,0,stream>>>(Y,Z,1.f,nullptr,nullptr,1.f/S);
  k_rowidft<<<S,D,0,stream>>>(Z,x,h,xb,a1);
  k_gemm<<<S,320,D*4,stream>>>(a1,w1,g1,D,D,0,nullptr);
  k_gemm<<<S,320,D*4,stream>>>(g1,w2,x1,D,D,1,xb);
  k_ln2<<<S,D,0,stream>>>(x1,a3);
  k_gemm<<<S,576,D*4,stream>>>(a3,w3,g3,D,F,0,nullptr);
  k_gemm<<<S,320,F*4,stream>>>(g3,w4,h2,F,D,2,nullptr);
  k_fin<<<S*D/256,256,0,stream>>>(h,x1,h2,(u16*)d_out);
}
```

```cpp
#include <hip/hip_runtime.h>
#include <stdint.h>
#include <type_traits>

typedef unsigned short u16;
typedef unsigned int u32;
typedef u32 u32x2 __attribute__((ext_vector_type(2)));
typedef u32 u32x4 __attribute__((ext_vector_type(4)));
typedef float f32x2 __attribute__((ext_vector_type(2)));
typedef float f32x4 __attribute__((ext_vector_type(4)));
typedef float f32x16 __attribute__((ext_vector_type(16)));
typedef __bf16 bf16x2 __attribute__((ext_vector_type(2)));
typedef __bf16 bf16x8 __attribute__((ext_vector_type(8)));
#define DI __device__ __forceinline__

namespace {
constexpr int S = 2048, D = 640, DF = 321, F = 1728, NCOL = 352;
constexpr size_t OFF_X = 0;
constexpr size_t OFF_WT = OFF_X + 352 * 8192;
constexpr size_t OFF_W1 = OFF_WT + 352 * 8192;
constexpr size_t OFF_W2 = OFF_W1 + 819200;
constexpr size_t OFF_W3 = OFF_W2 + 819200;
constexpr size_t OFF_W4 = OFF_W3 + 16 * 20 * 7 * 1024;
constexpr size_t OFF_H = OFF_W4 + 8 * 54 * 5 * 1024;
constexpr size_t ACT = (size_t)S * D * 2;
constexpr size_t OFF_XB = OFF_H + ACT;
constexpr size_t OFF_A1 = OFF_XB + ACT;
constexpr size_t OFF_G1 = OFF_A1 + ACT;
constexpr size_t OFF_X1 = OFF_G1 + ACT;
constexpr size_t OFF_G3 = OFF_X1 + ACT;
constexpr size_t OFF_ST = OFF_G3 + (size_t)S * F * 2;
constexpr size_t OFF_CSP = OFF_ST + 2048 * 8 * 8;
constexpr size_t OFF_CS = OFF_CSP + 20 * 1728 * 4;

template <int I, int N, class Fn> DI void sfor(Fn&& f) {
  if constexpr (I < N) { f(std::integral_constant<int, I>{}); sfor<I + 1, N>(f); }
}
DI float bflo(u32 v) { return __uint_as_float(v << 16); }
DI float bfhi(u32 v) { return __uint_as_float(v & 0xffff0000u); }
DI u32 pk(float lo, float hi) {
  f32x2 v = {lo, hi};
  return __builtin_bit_cast(u32, __builtin_convertvector(v, bf16x2));
}
DI float rbf(float x) { return bflo(pk(x, x)); }
DI f32x4 mfma16(u32x4 a, u32x4 b, f32x4 c) {
  return __builtin_amdgcn_mfma_f32_16x16x32_bf16(__builtin_bit_cast(bf16x8, a), __builtin_bit_cast(bf16x8, b), c, 0, 0, 0);
}
DI f32x16 mfma32(u32x4 a, u32x4 b, f32x16 c) {
  return __builtin_amdgcn_mfma_f32_32x32x16_bf16(__builtin_bit_cast(bf16x8, a), __builtin_bit_cast(bf16x8, b), c, 0, 0, 0);
}
DI float sinr(float rev) { return __builtin_amdgcn_sinf(rev); }
DI float cosr(float rev) { return __builtin_amdgcn_cosf(rev); }
template <int CTRL> DI float dpp(float x) {
  return __builtin_bit_cast(float, __builtin_amdgcn_mov_dpp(__builtin_bit_cast(int, x), CTRL, 0xf, 0xf, true));
}
DI float xrow_sum(float x) {
  auto s = __builtin_amdgcn_permlane16_swap(__float_as_uint(x), __float_as_uint(x), false, false);
  x = __uint_as_float(s[0]) + __uint_as_float(s[1]);
  auto t = __builtin_amdgcn_permlane32_swap(__float_as_uint(x), __float_as_uint(x), false, false);
  return __uint_as_float(t[0]) + __uint_as_float(t[1]);
}
DI float wave_sum(float x) {
  x += dpp<0xB1>(x);
  x += dpp<0x4E>(x);
  x += dpp<0x141>(x);
  x += dpp<0x140>(x);
  return xrow_sum(x);
}
DI void ln_scalars(float mean, float var, float& mb, float& rs) {
  mb = rbf(mean);
  float s = rbf(__builtin_amdgcn_sqrtf(rbf(rbf(var) + 1e-5f)));
  rs = __builtin_amdgcn_rcpf(s);
}
DI float gelu(float x) {
  const float c0 = -2.f * 0.7978845608028654f * 1.4426950408889634f, c1 = c0 * 0.044715f;
  float e = __builtin_amdgcn_exp2f(x * (c0 + c1 * x * x));
  return x * __builtin_amdgcn_rcpf(1.f + e);
}

template <int NT> DI void pretile_task(const u16* __restrict__ W, int N, int KS, int ks, int nb, char* dst, float* csp, int l) {
  int q = l >> 4, Ql = l & 15;
  int Q = nb * 16 + Ql;
  const u16* src = W + (size_t)(ks * 32 + q * 8) * N + Q * 4;
  u32x2 v[8];
#pragma unroll
  for (int j = 0; j < 8; ++j) v[j] = *(const u32x2*)(src + (size_t)j * N);
  int p = Q >> 3, w8 = Q & 7;
  bool solo = (p % NT) == (NT >> 1);
  int t = solo ? (w8 >> 2) : (w8 & 1);
  int a = solo ? (w8 & 3) : (w8 >> 1);
  int g = 2 * p + t, cb = g / NT, pos = g - cb * NT;
  int li = (cb & 1) ? (pos == 0 ? NT - 1 : pos - 1) : pos;
  char* o = dst + ((size_t)(cb * KS + ks) * NT + li) * 1024 + q * 256 + a * 64;
#pragma unroll
  for (int r = 0; r < 4; ++r) {
    u32x4 pc;
#pragma unroll
    for (int jj = 0; jj < 4; ++jj) {
      u32 lo = v[2 * jj][r >> 1], hi = v[2 * jj + 1][r >> 1];
      pc[jj] = __builtin_amdgcn_perm(hi, lo, (r & 1) ? 0x07060302u : 0x05040100u);
    }
    *(u32x4*)(o + r * 16) = pc;
  }
  if (csp) {
    f32x4 s4 = {0, 0, 0, 0};
#pragma unroll
    for (int j = 0; j < 8; ++j) { s4[0] += bflo(v[j][0]); s4[1] += bfhi(v[j][0]); s4[2] += bflo(v[j][1]); s4[3] += bfhi(v[j][1]); }
#pragma unroll
    for (int r = 0; r < 4; ++r) s4[r] = xrow_sum(s4[r]);
    if (q == 0) *(f32x4*)(csp + ks * N + Q * 4) = s4;
  }
}

template <bool FWD> DI u32x4 radix8_const(int l, int s2) {
  int i = l & 31, h = l >> 5;
  int reg = (i & 3) + 4 * ((i >> 3) & 1);
  int o = 4 * ((i >> 2) & 1) + (reg >> 1), c = reg & 1;
  u32x4 r;
#pragma unroll
  for (int jj = 0; jj < 4; ++jj) {
    int in = 4 * h + jj;
    int u1 = FWD ? o : in, s1 = FWD ? in : o;
    float a = (float)(u1 * (256 * s1 + s2)) * (1.f / 2048.f);
    float cs = cosr(a), sn = sinr(a);
    u32 v = c ? pk(-sn, cs) : pk(cs, sn);
    r[jj] = (i < 16) ? v : 0u;
  }
  return r;
}

constexpr int K1_TS = 1040;
__global__ __launch_bounds__(512) void k1_fwd(const u16* __restrict__ x, const u16* __restrict__ wr, const u16* __restrict__ wi, char* __restrict__ ws) {
  __shared__ __attribute__((aligned(16))) char lds[11 * K1_TS];
  const int b = blockIdx.x, s2 = (b & 7) * 32 + (b >> 3);
  const int tid = threadIdx.x, w = __builtin_amdgcn_readfirstlane(tid >> 6), l = tid & 63, m = l & 15, q = l >> 4;
  const int s = 256 * w + s2;
  u32 xv[5];
#pragma unroll
  for (int j = 0; j < 5; ++j) xv[j] = *(const u32*)(x + s * D + 32 * (5 * q + j) + 2 * m);
  if (tid < NCOL) {
    int k2 = tid / 11, k1 = tid - k2 * 11, k = k1 + 20 * k2;
    bool mir = k > 320;
    int kk = mir ? 640 - k : k;
    int wv = s2 >> 6, ql = (s2 >> 4) & 3, cl = s2 & 15;
    u32 o[8];
#pragma unroll
    for (int e = 0; e < 8; ++e) {
      int u = 2 * wv + (e >> 2) + 8 * cl + 512 * ql + 128 * (e & 3);
      int uu = mir ? ((S - u) & (S - 1)) : u;
      u32 a = wr[uu * DF + kk], c = wi[uu * DF + kk];
      o[e] = a | ((c ^ (mir ? 0x8000u : 0u)) << 16);
    }
    char* dst = ws + OFF_WT + (size_t)(k1 * 32 + k2) * 8192 + s2 * 32;
    *(u32x4*)dst = u32x4{o[0], o[1], o[2], o[3]};
    *(u32x4*)(dst + 16) = u32x4{o[4], o[5], o[6], o[7]};
  }
  u32x4 f20c, f20s;
  {
    float c[5], sn[5];
#pragma unroll
    for (int j = 0; j < 5; ++j) { float a = (float)((5 * q + j) * m) * (1.f / 20.f); c[j] = cosr(a); sn[j] = -sinr(a); }
    f20c = u32x4{pk(c[0], c[1]), pk(c[2], c[3]), pk(c[4], 0.f), 0u};
    f20s = u32x4{pk(sn[0], sn[1]), pk(sn[2], sn[3]), pk(sn[4], 0.f), 0u};
  }
  float twc[8], tws[8];
#pragma unroll
  for (int j = 0; j < 8; ++j) { float a = (float)((8 * q + j) * m) * (1.f / 640.f); twc[j] = cosr(a); tws[j] = sinr(a); }
  u32x4 f32c[2], f32s[2];
#pragma unroll
  for (int jj = 0; jj < 4; ++jj) {
    float a0 = (float)((8 * q + 2 * jj) * m) * (1.f / 32.f), a1 = (float)((8 * q + 2 * jj + 1) * m) * (1.f / 32.f);
    f32c[0][jj] = pk(cosr(a0), cosr(a1)); f32s[0][jj] = pk(sinr(a0), sinr(a1));
    f32c[1][jj] = f32c[0][jj] ^ 0x80000000u; f32s[1][jj] = f32s[0][jj] ^ 0x80000000u;
  }
  const u32x4 r8 = radix8_const<true>(l, s2);
  float v[10];
#pragma unroll
  for (int j = 0; j < 5; ++j) { v[2 * j] = bflo(xv[j]); v[2 * j + 1] = bfhi(xv[j]); }
  float sum = 0;
#pragma unroll
  for (int j = 0; j < 10; ++j) sum += v[j];
  float mean = wave_sum(sum) * (1.f / D);
  float sq = 0;
#pragma unroll
  for (int j = 0; j < 10; ++j) { float d = v[j] - mean; sq += d * d; }
  float var = wave_sum(sq) * (1.f / D);
  float mb, rs;
  ln_scalars(mean, var, mb, rs);
#pragma unroll
  for (int j = 0; j < 10; ++j) v[j] = rbf(v[j] - mb) * rs;
  u32x4 a0 = {pk(v[0], v[2]), pk(v[4], v[6]), pk(v[8], 0.f), 0u};
  u32x4 a1 = {pk(v[1], v[3]), pk(v[5], v[7]), pk(v[9], 0.f), 0u};
  const f32x4 z4 = {0, 0, 0, 0};
  f32x4 pr0 = mfma16(a0, f20c, z4), pi0 = mfma16(a0, f20s, z4), pr1 = mfma16(a1, f20c, z4), pi1 = mfma16(a1, f20s, z4);
  u32x4 br, bi, bn;
#pragma unroll
  for (int r = 0; r < 4; ++r) {
    float r0 = pr0[r] * twc[2 * r] + pi0[r] * tws[2 * r], i0 = pi0[r] * twc[2 * r] - pr0[r] * tws[2 * r];
    float r1 = pr1[r] * twc[2 * r + 1] + pi1[r] * tws[2 * r + 1], i1 = pi1[r] * twc[2 * r + 1] - pr1[r] * tws[2 * r + 1];
    br[r] = pk(r0, r1); bi[r] = pk(i0, i1); bn[r] = br[r] ^ 0x80008000u;
  }
#pragma unroll
  for (int t = 0; t < 2; ++t) {
    f32x4 yr = mfma16(f32s[t], bi, mfma16(f32c[t], br, z4));
    f32x4 yi = mfma16(f32s[t], bn, mfma16(f32c[t], bi, z4));
    if (m < 11) {
#pragma unroll
      for (int r = 0; r < 4; ++r)
        *(u32*)(lds + m * K1_TS + (w >> 2) * 512 + (16 * t + 4 * q + r) * 16 + (w & 3) * 4) = pk(yr[r], yi[r]);
    }
  }
  __syncthreads();
  f32x16 z16;
#pragma unroll
  for (int i = 0; i < 16; ++i) z16[i] = 0.f;
  for (int k1 = w; k1 < 11; k1 += 8) {
    u32x4 bb = *(const u32x4*)(lds + k1 * K1_TS + l * 16);
    f32x16 o = mfma32(r8, bb, z16);
    u32x4 ov = {pk(o[0], o[1]), pk(o[2], o[3]), pk(o[4], o[5]), pk(o[6], o[7])};
    *(u32x4*)(ws + OFF_X + (size_t)(k1 * 32 + (l & 31)) * 8192 + s2 * 32 + (l >> 5) * 16) = ov;
  }
}

__global__ __launch_bounds__(256) void k2_col(const u16* __restrict__ w1, const u16* __restrict__ w2, const u16* __restrict__ w3,
                                              const u16* __restrict__ w4, char* __restrict__ ws) {
  const int tid = threadIdx.x, w = __builtin_amdgcn_readfirstlane(tid >> 6), l = tid & 63, m = l & 15, q = l >> 4;
  if (blockIdx.x >= NCOL) {
    int t = (blockIdx.x - NCOL) * 4 + w;
    if (t < 200) pretile_task<5>(w1, D, 20, t / 10, t % 10, ws + OFF_W1, nullptr, l);
    else if (t < 400) { t -= 200; pretile_task<5>(w2, D, 20, t / 10, t % 10, ws + OFF_W2, nullptr, l); }
    else if (t < 940) { t -= 400; pretile_task<7>(w3, F, 20, t / 27, t % 27, ws + OFF_W3, (float*)(ws + OFF_CSP), l); }
    else { t -= 940; pretile_task<5>(w4, D, 54, t / 10, t % 10, ws + OFF_W4, nullptr, l); }
    return;
  }
  const int col = blockIdx.x;
  char* xp = ws + OFF_X + (size_t)col * 8192 + (64 * q + m) * 32 + 8 * w;
  u32x2 in[4];
#pragma unroll
  for (int i = 0; i < 4; ++i) in[i] = *(const u32x2*)(xp + i * 512);
  const char* wp = ws + OFF_WT + (size_t)col * 8192 + w * 2048 + l * 32;
  u32x4 wv[2] = {*(const u32x4*)wp, *(const u32x4*)(wp + 16)};
  u32x4 fre, fim;
  float twc[4], tws[4];
#pragma unroll
  for (int r = 0; r < 4; ++r) {
    float a = (float)((4 * q + r) * m);
    float c = cosr(a * (1.f / 16.f)), sn = sinr(a * (1.f / 16.f));
    fre[r] = pk(c, sn); fim[r] = pk(-sn, c);
    twc[r] = cosr(a * (1.f / 256.f)); tws[r] = sinr(a * (1.f / 256.f));
  }
  const f32x4 z4 = {0, 0, 0, 0};
  u32x2 out[4];
#pragma unroll
  for (int t = 0; t < 2; ++t) {
    u32x4 a = {in[0][t], in[1][t], in[2][t], in[3][t]};
    f32x4 pr = mfma16(a, fre, z4), pi = mfma16(a, fim, z4);
#pragma unroll
    for (int r = 0; r < 4; ++r) a[r] = pk(pr[r] * twc[r] + pi[r] * tws[r], pi[r] * twc[r] - pr[r] * tws[r]);
    pr = mfma16(fre, a, z4); pi = mfma16(fim, a, z4);
#pragma unroll
    for (int r = 0; r < 4; ++r) {
      float wr_ = bflo(wv[t][r]), wi_ = bfhi(wv[t][r]);
      a[r] = pk(pr[r] * wr_ - pi[r] * wi_, -(pr[r] * wi_ + pi[r] * wr_));
    }
    pr = mfma16(a, fre, z4); pi = mfma16(a, fim, z4);
#pragma unroll
    for (int r = 0; r < 4; ++r) a[r] = pk(pr[r] * twc[r] + pi[r] * tws[r], pi[r] * twc[r] - pr[r] * tws[r]);
    pr = mfma16(fre, a, z4); pi = mfma16(fim, a, z4);
#pragma unroll
    for (int r = 0; r < 4; ++r) out[r][t] = pk(pr[r], pi[r]);
  }
#pragma unroll
  for (int i = 0; i < 4; ++i) *(u32x2*)(xp + i * 512) = out[i];
}

constexpr int K3_KS = 144, K3_RS = 11 * 144;
__global__ __launch_bounds__(512) void k3_inv(const u16* __restrict__ x, char* __restrict__ ws) {
  __shared__ __attribute__((aligned(16))) char lds[8 * K3_RS];
  const int b = blockIdx.x, s2 = (b & 7) * 32 + (b >> 3);
  const int tid = threadIdx.x, w = __builtin_amdgcn_readfirstlane(tid >> 6), l = tid & 63, m = l & 15, q = l >> 4;
  const int s = 256 * w + s2;
  const char* zp = ws + OFF_X + (size_t)(l & 31) * 8192 + s2 * 32 + (l >> 5) * 16;
  u32x4 zb0 = *(const u32x4*)(zp + (size_t)w * 32 * 8192), zb1 = {0, 0, 0, 0};
  if (w < 3) zb1 = *(const u32x4*)(zp + (size_t)(w + 8) * 32 * 8192);
  u32x4 xv0 = *(const u32x4*)(x + s * D + 32 * m + 8 * q), xv1 = {0, 0, 0, 0};
  if (m < 4) xv1 = *(const u32x4*)(x + s * D + 32 * (16 + m) + 8 * q);
  if (b < 4 && tid < 432) {
    int Q = b * 108 + (tid >> 2), r = tid & 3;
    if ((tid >> 2) < 108) {
      const float* csp = (const float*)(ws + OFF_CSP) + Q * 4 + r;
      float sacc = 0;
#pragma unroll
      for (int i = 0; i < 20; ++i) sacc += csp[i * F];
      int p = Q >> 3, w8 = Q & 7;
      bool solo = (p % 7) == 3;
      int t = solo ? (w8 >> 2) : (w8 & 1), a = solo ? (w8 & 3) : (w8 >> 1);
      int g = 2 * p + t, cb = g / 7, pos = g - cb * 7;
      int li = (cb & 1) ? (pos == 0 ? 6 : pos - 1) : pos;
      ((float*)(ws + OFF_CS))[(cb * 7 + li) * 16 + 4 * a + r] = sacc;
    }
  }
  const u32x4 r8 = radix8_const<false>(l, s2);
  u32x4 gre[2][2], gim[2][2];
  float twc[2][4], tws[2][4];
#pragma unroll
  for (int t = 0; t < 2; ++t) {
    int n2 = 8 * (m >> 2) + 4 * t + (m & 3);
    u32 sg = (n2 & 1) ? 0x80008000u : 0u;
#pragma unroll
    for (int jj = 0; jj < 4; ++jj) {
      float a = (float)((4 * q + jj) * n2);
      float c = cosr(a * (1.f / 32.f)), sn = sinr(a * (1.f / 32.f));
      gre[t][0][jj] = pk(c, sn); gim[t][0][jj] = pk(-sn, c);
      gre[t][1][jj] = gre[t][0][jj] ^ sg; gim[t][1][jj] = gim[t][0][jj] ^ sg;
      const float sc = 1.f / ((float)S * (float)D);
      twc[t][jj] = cosr(a * (1.f / 640.f)) * sc; tws[t][jj] = sinr(a * (1.f / 640.f)) * sc;
    }
  }
  u32x4 g20[2];
#pragma unroll
  for (int nt = 0; nt < 2; ++nt)
#pragma unroll
    for (int jj = 0; jj < 4; ++jj) {
      int k1 = 4 * q + jj;
      float a = (float)(k1 * (16 * nt + m)) * (1.f / 20.f);
      float wg = (k1 == 0 || k1 == 10) ? 1.f : (k1 < 10 ? 2.f : 0.f);
      float sw = (k1 > 0 && k1 < 10) ? 2.f : 0.f;
      g20[nt][jj] = pk(wg * cosr(a), sw * sinr(a));
    }
  f32x16 z16;
#pragma unroll
  for (int i = 0; i < 16; ++i) z16[i] = 0.f;
  {
    f32x16 o = mfma32(r8, zb0, z16);
#pragma unroll
    for (int i = 0; i < 4; ++i) *(u32*)(lds + (4 * (l >> 5) + i) * K3_RS + w * K3_KS + (l & 31) * 4) = pk(o[2 * i], o[2 * i + 1]);
    if (w < 3) {
      o = mfma32(r8, zb1, z16);
#pragma unroll
      for (int i = 0; i < 4; ++i) *(u32*)(lds + (4 * (l >> 5) + i) * K3_RS + (w + 8) * K3_KS + (l & 31) * 4) = pk(o[2 * i], o[2 * i + 1]);
    }
  }
  __syncthreads();
  const char* lp = lds + w * K3_RS + (m < 11 ? m : 10) * K3_KS + q * 16;
  u32x4 za0 = *(const u32x4*)lp, za1 = *(const u32x4*)(lp + 64);
  const f32x4 z4 = {0, 0, 0, 0};
  float y[16];
#pragma unroll
  for (int t = 0; t < 2; ++t) {
    f32x4 qr = mfma16(za1, gre[t][1], mfma16(za0, gre[t][0], z4));
    f32x4 qi = mfma16(za1, gim[t][1], mfma16(za0, gim[t][0], z4));
    u32x4 a;
#pragma unroll
    for (int r = 0; r < 4; ++r) a[r] = pk(qr[r] * twc[t][r] + qi[r] * tws[t][r], qi[r] * twc[t][r] - qr[r] * tws[t][r]);
    f32x4 y0 = mfma16(a, g20[0], z4), y1 = mfma16(a, g20[1], z4);
#pragma unroll
    for (int r = 0; r < 4; ++r) { y[4 * t + r] = y0[r]; y[8 + 4 * t + r] = y1[r]; }
  }
  const bool v1 = m < 4;
  float h[16];
  u32x4 hp0, hp1;
#pragma unroll
  for (int j = 0; j < 4; ++j) {
    u32 yb = pk(y[2 * j], y[2 * j + 1]);
    hp0[j] = pk(bflo(xv0[j]) + bflo(yb), bfhi(xv0[j]) + bfhi(yb));
    yb = pk(y[8 + 2 * j], y[8 + 2 * j + 1]);
    hp1[j] = v1 ? pk(bflo(xv1[j]) + bflo(yb), bfhi(xv1[j]) + bfhi(yb)) : 0u;
    h[2 * j] = bflo(hp0[j]); h[2 * j + 1] = bfhi(hp0[j]); h[8 + 2 * j] = bflo(hp1[j]); h[8 + 2 * j + 1] = bfhi(hp1[j]);
  }
  const size_t fo = (size_t)(s2 >> 1) * 20480 + m * 1024 + q * 256 + ((s2 & 1) * 8 + w) * 16;
  *(u32x4*)(ws + OFF_H + fo) = hp0;
  if (v1) *(u32x4*)(ws + OFF_H + fo + 16384) = hp1;
#pragma unroll
  for (int pass = 0; pass < 2; ++pass) {
    float sum = 0;
#pragma unroll
    for (int j = 0; j < 16; ++j) sum += h[j];
    float mean = wave_sum(sum) * (1.f / D);
    float sq = 0, sq1 = 0;
#pragma unroll
    for (int j = 0; j < 8; ++j) { float d = h[j] - mean; sq += d * d; d = h[8 + j] - mean; sq1 += d * d; }
    float var = wave_sum(sq + (v1 ? sq1 : 0.f)) * (1.f / D);
    float mb, rs;
    ln_scalars(mean, var, mb, rs);
#pragma unroll
    for (int j = 0; j < 4; ++j) {
      u32 d0 = pk(h[2 * j] - mb, h[2 * j + 1] - mb), d1 = pk(h[8 + 2 * j] - mb, h[8 + 2 * j + 1] - mb);
      hp0[j] = pk(bflo(d0) * rs, bfhi(d0) * rs);
      hp1[j] = v1 ? pk(bflo(d1) * rs, bfhi(d1) * rs) : 0u;
      h[2 * j] = bflo(hp0[j]); h[2 * j + 1] = bfhi(hp0[j]); h[8 + 2 * j] = bflo(hp1[j]); h[8 + 2 * j + 1] = bfhi(hp1[j]);
    }
    char* o = ws + (pass ? OFF_A1 : OFF_XB) + fo;
    *(u32x4*)o = hp0;
    if (v1) *(u32x4*)(o + 16384) = hp1;
  }
}

template <int IMM> DI void gld16(u32x4& d, const void* sbase, u32 voff) {
  asm volatile("global_load_dwordx4 %0, %1, %2 offset:%3" : "=v"(d) : "v"(voff), "s"(sbase), "i"(IMM));
}
template <int N> DI void glds(u32 ldsaddr, const void* sbase, u32 voff) {
  if constexpr (N == 1)
    asm volatile("s_mov_b32 m0, %0\n\ts_nop 0\n\tglobal_load_lds_dwordx4 %1, %2" ::"s"(ldsaddr), "v"(voff), "s"(sbase) : "memory");
  else if constexpr (N == 2)
    asm volatile("s_mov_b32 m0, %0\n\ts_nop 0\n\tglobal_load_lds_dwordx4 %1, %2\n\tglobal_load_lds_dwordx4 %1, %2 offset:1024" ::"s"(ldsaddr), "v"(voff), "s"(sbase) : "memory");
  else if constexpr (N == 3)
    asm volatile("s_mov_b32 m0, %0\n\ts_nop 0\n\tglobal_load_lds_dwordx4 %1, %2\n\tglobal_load_lds_dwordx4 %1, %2 offset:1024\n\tglobal_load_lds_dwordx4 %1, %2 offset:2048" ::"s"(ldsaddr), "v"(voff), "s"(sbase) : "memory");
  else
    asm volatile("s_mov_b32 m0, %0\n\ts_nop 0\n\tglobal_load_lds_dwordx4 %1, %2\n\tglobal_load_lds_dwordx4 %1, %2 offset:1024\n\tglobal_load_lds_dwordx4 %1, %2 offset:2048\n\tglobal_load_lds_dwordx4 %1, %2 offset:3072" ::"s"(ldsaddr), "v"(voff), "s"(sbase) : "memory");
}
template <int N> DI void glds_n(u32 ldsaddr, const char* sbase, u32 voff) {
  sfor<0, (N + 3) / 4>([&](auto I) {
    constexpr int i = I.value, c = (N - 4 * i) < 4 ? (N - 4 * i) : 4;
    glds<c>(ldsaddr + i * 4096, sbase + i * 4096, voff);
  });
}
template <int N> DI void wait_vm() { asm volatile("s_waitcnt vmcnt(%0)" ::"i"(N) : "memory"); }
DI void tie(u32x4& a) { asm volatile("" : "+v"(a)); }
DI void bar() { asm volatile("s_barrier" ::: "memory"); }

template <int NT> DI int pair_kb(int cb, int pp) { return (cb >> 1) * NT + ((cb & 1) ? (NT / 2 + 1) : 0) + pp; }
template <int NT> DI int solo_off(int cb, int m, int q) { return ((cb >> 1) * NT + NT / 2) * 1024 + (2 * (cb & 1) + (q >> 1)) * 256 + m * 16 + (q & 1) * 8; }

template <int MODE> __global__ __launch_bounds__(256) void k_gemm12(char* __restrict__ ws) {
  extern __shared__ __attribute__((aligned(16))) char lds[];
  const int b = blockIdx.x, xcd = b & 7, i5 = b >> 3, tb = xcd * 4 + (i5 >> 3), cb = i5 & 7;
  const int tid = threadIdx.x, w = __builtin_amdgcn_readfirstlane(tid >> 6), l = tid & 63, m = l & 15, q = l >> 4;
  const int T = tb * 4 + w;
  const char* wsrc = ws + (MODE == 0 ? OFF_W1 : OFF_W2) + (size_t)cb * 102400;
  const u32 lbase = (u32)(size_t)lds;
  glds_n<25>(lbase + w * 25600, wsrc + w * 25600, l * 16);
  u32x4 act[20];
  if constexpr (MODE == 0) {
    const char* ap = ws + OFF_A1 + (size_t)T * 20480 + l * 16;
#pragma unroll
    for (int k = 0; k < 20; ++k) act[k] = *(const u32x4*)(ap + k * 1024);
  } else {
    const int rho = T * 16 + m, s = (rho & 7) * 256 + (rho >> 3);
#pragma unroll
    for (int g = 0; g < 4; ++g) {
      const int sh = g == 0 ? 1 : g == 1 ? -1 : g == 2 ? 2 : -2;
      int sp = s - sh;
      bool ok = sp >= 0 && sp < S;
      int rp = ok ? (sp & 255) * 8 + (sp >> 8) : rho;
      const char* ap = ws + OFF_G1 + (size_t)(rp >> 4) * 20480 + q * 256 + (rp & 15) * 16 + g * 5120;
#pragma unroll
      for (int k = 0; k < 5; ++k) {
        u32x4 v = *(const u32x4*)(ap + k * 1024);
        act[g * 5 + k] = ok ? v : u32x4{0, 0, 0, 0};
      }
    }
  }
  wait_vm<20>();
  bar();
  f32x4 acc[5];
#pragma unroll
  for (int i = 0; i < 5; ++i) acc[i] = f32x4{0, 0, 0, 0};
#pragma unroll
  for (int k = 0; k < 20; ++k)
#pragma unroll
    for (int i = 0; i < 5; ++i) acc[i] = mfma16(*(const u32x4*)(lds + (k * 5 + i) * 1024 + l * 16), act[k], acc[i]);
  const size_t tbase = (size_t)T * 20480;
  const size_t po0 = tbase + pair_kb<5>(cb, 0) * 1024 + l * 16, so = tbase + solo_off<5>(cb, m, q);
  u32 o[10];
  if constexpr (MODE == 0) {
#pragma unroll
    for (int i = 0; i < 5; ++i)
#pragma unroll
      for (int hh = 0; hh < 2; ++hh) {
        u32 p = pk(acc[i][2 * hh], acc[i][2 * hh + 1]);
        o[2 * i + hh] = pk(gelu(bflo(p)), gelu(bfhi(p)));
      }
    char* op = ws + OFF_G1;
    *(u32x4*)(op + po0) = u32x4{o[0], o[1], o[2], o[3]};
    *(u32x4*)(op + po0 + 1024) = u32x4{o[4], o[5], o[6], o[7]};
    *(u32x2*)(op + so) = u32x2{o[8], o[9]};
  } else {
    const char* rp = ws + OFF_XB;
    u32x4 r0 = *(const u32x4*)(rp + po0), r1 = *(const u32x4*)(rp + po0 + 1024);
    u32x2 r2 = *(const u32x2*)(rp + so);
    u32 rr[10] = {r0[0], r0[1], r0[2], r0[3], r1[0], r1[1], r1[2], r1[3], r2[0], r2[1]};
    float sum = 0;
#pragma unroll
    for (int i = 0; i < 5; ++i)
#pragma unroll
      for (int hh = 0; hh < 2; ++hh) {
        u32 p = pk(acc[i][2 * hh], acc[i][2 * hh + 1]);
        u32 xo = pk(bflo(rr[2 * i + hh]) + bflo(p), bfhi(rr[2 * i + hh]) + bfhi(p));
        o[2 * i + hh] = xo;
        sum += bflo(xo) + bfhi(xo);
      }
    char* op = ws + OFF_X1;
    *(u32x4*)(op + po0) = u32x4{o[0], o[1], o[2], o[3]};
    *(u32x4*)(op + po0 + 1024) = u32x4{o[4], o[5], o[6], o[7]};
    *(u32x2*)(op + so) = u32x2{o[8], o[9]};
    float mean = xrow_sum(sum) * (1.f / 80.f), sq = 0;
#pragma unroll
    for (int i = 0; i < 10; ++i) { float d0 = bflo(o[i]) - mean, d1 = bfhi(o[i]) - mean; sq += d0 * d0 + d1 * d1; }
    sq = xrow_sum(sq);
    if (q == 0) *(f32x2*)(ws + OFF_ST + ((size_t)(T * 16 + m) * 8 + cb) * 8) = f32x2{mean, sq};
  }
}

__global__ __launch_bounds__(512) void k_gemm3(char* __restrict__ ws) {
  extern __shared__ __attribute__((aligned(16))) char lds[];
  const int b = blockIdx.x, xcd = b & 7, i5 = b >> 3, tb = xcd * 2 + (i5 >> 4), cb = i5 & 15;
  const int tid = threadIdx.x, w = __builtin_amdgcn_readfirstlane(tid >> 6), l = tid & 63, m = l & 15, q = l >> 4;
  const int T = tb * 8 + w;
  const char* wsrc = ws + OFF_W3 + (size_t)cb * 143360;
  const char* asrc = ws + OFF_X1 + (size_t)T * 20480;
  const u32 lbase = (u32)(size_t)lds;
  u32x4 act[20];
  const f32x4* stp = (const f32x4*)(ws + OFF_ST + (size_t)(T * 16 + m) * 64);
  f32x4 st0 = stp[0], st1 = stp[1], st2 = stp[2], st3 = stp[3];
  f32x4 cs[7];
#pragma unroll
  for (int i = 0; i < 7; ++i) cs[i] = *(const f32x4*)(ws + OFF_CS + ((cb * 7 + i) * 16 + 4 * q) * 4);
  const int c0 = w < 4 ? 4 * w : 16 + 3 * (w - 4);
  sfor<0, 5>([&](auto SI) {
    constexpr int st = SI.value;
    glds<3>(lbase + (st * 28 + c0) * 1024, wsrc + (st * 28 + c0) * 1024, l * 16);
    if (w < 4) glds<1>(lbase + (st * 28 + c0 + 3) * 1024, wsrc + (st * 28 + c0 + 3) * 1024, l * 16);
    sfor<0, 4>([&](auto KI) { gld16<KI.value * 1024>(act[st * 4 + KI.value], asrc + st * 4096, l * 16); });
  });
  f32x4 acc[7];
#pragma unroll
  for (int i = 0; i < 7; ++i) acc[i] = f32x4{0, 0, 0, 0};
  sfor<0, 5>([&](auto SI) {
    constexpr int st = SI.value;
    wait_vm<(4 - st) * 7>();
    sfor<0, 4>([&](auto KI) { tie(act[st * 4 + KI.value]); });
    bar();
#pragma unroll
    for (int k = st * 4; k < st * 4 + 4; ++k)
#pragma unroll
      for (int i = 0; i < 7; ++i) acc[i] = mfma16(*(const u32x4*)(lds + (k * 7 + i) * 1024 + l * 16), act[k], acc[i]);
  });
  float mean = (st0[0] + st0[2] + st1[0] + st1[2] + st2[0] + st2[2] + st3[0] + st3[2]) * 0.125f;
  float m2 = st0[1] + st0[3] + st1[1] + st1[3] + st2[1] + st2[3] + st3[1] + st3[3], dd = 0;
  { float d;
    d = st0[0] - mean; dd += d * d; d = st0[2] - mean; dd += d * d; d = st1[0] - mean; dd += d * d; d = st1[2] - mean; dd += d * d;
    d = st2[0] - mean; dd += d * d; d = st2[2] - mean; dd += d * d; d = st3[0] - mean; dd += d * d; d = st3[2] - mean; dd += d * d; }
  float mb, rs;
  ln_scalars(mean, (m2 + 80.f * dd) * (1.f / D), mb, rs);
  const float mrs = -mb * rs;
  u32 o[14];
#pragma unroll
  for (int i = 0; i < 7; ++i)
#pragma unroll
    for (int hh = 0; hh < 2; ++hh) {
      u32 p = pk(acc[i][2 * hh] * rs + mrs * cs[i][2 * hh], acc[i][2 * hh + 1] * rs + mrs * cs[i][2 * hh + 1]);
      o[2 * i + hh] = pk(gelu(bflo(p)), gelu(bfhi(p)));
    }
  char* op = ws + OFF_G3 + (size_t)T * 55296;
  const int po0 = pair_kb<7>(cb, 0) * 1024 + l * 16;
  *(u32x4*)(op + po0) = u32x4{o[0], o[1], o[2], o[3]};
  if (cb < 15) {
    *(u32x4*)(op + po0 + 1024) = u32x4{o[4], o[5], o[6], o[7]};
    *(u32x4*)(op + po0 + 2048) = u32x4{o[8], o[9], o[10], o[11]};
  }
  *(u32x2*)(op + solo_off<7>(cb, m, q)) = u32x2{o[12], o[13]};
}

__global__ __launch_bounds__(512) void k_gemm4(char* __restrict__ ws, u16* __restrict__ out) {
  extern __shared__ __attribute__((aligned(16))) char lds[];
  const int b = blockIdx.x, xcd = b & 7, i5 = b >> 3, tb = xcd * 4 + (i5 >> 3), cb = i5 & 7;
  const int tid = threadIdx.x, w = __builtin_amdgcn_readfirstlane(tid >> 6), l = tid & 63, m = l & 15, q = l >> 4;
  const int kh = w >> 2, wq = w & 3, T = tb * 4 + wq;
  const char* wsrc = ws + OFF_W4 + (size_t)cb * 276480 + kh * 138240;
  const char* asrc = ws + OFF_G3 + (size_t)T * 55296 + kh * 27648;
  const u32 lbase = (u32)(size_t)lds + kh * 76800;
  const char* lrd = lds + kh * 76800 + l * 16;
  u32x4 act[27];
  auto issue_w = [&](auto SI) {
    constexpr int st = SI.value;
    glds<3>(lbase + (st % 5) * 15360 + wq * 4096, wsrc + st * 15360 + wq * 4096, l * 16);
    if (wq < 3) glds<1>(lbase + (st % 5) * 15360 + wq * 4096 + 3072, wsrc + st * 15360 + wq * 4096 + 3072, l * 16);
  };
  auto issue_a = [&](auto SI) {
    constexpr int st = SI.value;
    sfor<0, 3>([&](auto KI) { gld16<KI.value * 1024>(act[st * 3 + KI.value], asrc + st * 3072, l * 16); });
  };
  sfor<0, 5>([&](auto SI) { issue_w(SI); issue_a(SI); });
  f32x4 acc[5];
#pragma unroll
  for (int i = 0; i < 5; ++i) acc[i] = f32x4{0, 0, 0, 0};
  sfor<0, 9>([&](auto SI) {
    constexpr int st = SI.value;
    constexpr int last = (st == 0) ? 4 : (st + 3 < 8 ? st + 3 : 8);
    wait_vm<(last - st) * 6>();
    sfor<0, 3>([&](auto KI) { tie(act[st * 3 + KI.value]); });
    asm volatile("s_waitcnt lgkmcnt(0)" ::: "memory");
    bar();
    if constexpr (st >= 1 && st + 4 < 9) {
      issue_w(std::integral_constant<int, st + 4>{});
      issue_a(std::integral_constant<int, st + 4>{});
    }
#pragma unroll
    for (int k = 0; k < 3; ++k)
#pragma unroll
      for (int i = 0; i < 5; ++i) acc[i] = mfma16(*(const u32x4*)(lrd + (st % 5) * 15360 + (k * 5 + i) * 1024), act[st * 3 + k], acc[i]);
  });
  __syncthreads();
  if (kh == 1) {
#pragma unroll
    for (int i = 0; i < 5; ++i) *(f32x4*)(lds + (wq * 5 + i) * 1024 + l * 16) = acc[i];
  }
  __syncthreads();
  if (kh == 1) return;
#pragma unroll
  for (int i = 0; i < 5; ++i) acc[i] += *(const f32x4*)(lds + (wq * 5 + i) * 1024 + l * 16);
  const size_t tbase = (size_t)T * 20480;
  const int kb0 = pair_kb<5>(cb, 0);
  const size_t po0 = tbase + kb0 * 1024 + l * 16, so = tbase + solo_off<5>(cb, m, q);
  u32x4 a0 = *(const u32x4*)(ws + OFF_X1 + po0), a1 = *(const u32x4*)(ws + OFF_X1 + po0 + 1024);
  u32x2 a2 = *(const u32x2*)(ws + OFF_X1 + so);
  u32x4 h0 = *(const u32x4*)(ws + OFF_H + po0), h1 = *(const u32x4*)(ws + OFF_H + po0 + 1024);
  u32x2 h2 = *(const u32x2*)(ws + OFF_H + so);
  u32 xa[10] = {a0[0], a0[1], a0[2], a0[3], a1[0], a1[1], a1[2], a1[3], a2[0], a2[1]};
  u32 ha[10] = {h0[0], h0[1], h0[2], h0[3], h1[0], h1[1], h1[2], h1[3], h2[0], h2[1]};
  u32 o[10];
#pragma unroll
  for (int i = 0; i < 5; ++i)
#pragma unroll
    for (int hh = 0; hh < 2; ++hh) {
      int e = 2 * i + hh;
      u32 p = pk(acc[i][2 * hh], acc[i][2 * hh + 1]);
      u32 t = pk(bflo(xa[e]) + bflo(p), bfhi(xa[e]) + bfhi(p));
      o[e] = pk(bflo(ha[e]) + bflo(t), bfhi(ha[e]) + bfhi(t));
    }
  const int rho = T * 16 + m, s = (rho & 7) * 256 + (rho >> 3);
  u16* op = out + (size_t)s * D;
  *(u32x4*)(op + kb0 * 32 + q * 8) = u32x4{o[0], o[1], o[2], o[3]};
  *(u32x4*)(op + kb0 * 32 + 32 + q * 8) = u32x4{o[4], o[5], o[6], o[7]};
  *(u32x2*)(op + ((cb >> 1) * 5 + 2) * 32 + (cb & 1) * 16 + q * 4) = u32x2{o[8], o[9]};
}
}

extern "C" void kernel_launch(void* const* d_in, const int* in_sizes, int n_in, void* d_out, int out_size, void* d_ws, size_t ws_size,
                              hipStream_t stream) {
  const u16* x = (const u16*)d_in[0];
  char* ws = (char*)d_ws;
  (void)hipFuncSetAttribute((const void*)k_gemm12<0>, hipFuncAttributeMaxDynamicSharedMemorySize, 102400);
  (void)hipFuncSetAttribute((const void*)k_gemm12<1>, hipFuncAttributeMaxDynamicSharedMemorySize, 102400);
  (void)hipFuncSetAttribute((const void*)k_gemm3, hipFuncAttributeMaxDynamicSharedMemorySize, 143360);
  (void)hipFuncSetAttribute((const void*)k_gemm4, hipFuncAttributeMaxDynamicSharedMemorySize, 153600);
  k1_fwd<<<256, 512, 0, stream>>>(x, (const u16*)d_in[1], (const u16*)d_in[2], ws);
  k2_col<<<NCOL + 370, 256, 0, stream>>>((const u16*)d_in[4], (const u16*)d_in[5], (const u16*)d_in[6], (const u16*)d_in[7], ws);
  k3_inv<<<256, 512, 0, stream>>>(x, ws);
  k_gemm12<0><<<256, 256, 102400, stream>>>(ws);
  k_gemm12<1><<<256, 256, 102400, stream>>>(ws);
  k_gemm3<<<256, 512, 143360, stream>>>(ws);
  k_gemm4<<<256, 512, 153600, stream>>>(ws, (u16*)d_out);
}
```

```cpp
#include <hip/hip_runtime.h>
#include <stdint.h>
#include <type_traits>

typedef unsigned short u16;
typedef unsigned int u32;
typedef u32 u32x2 __attribute__((ext_vector_type(2)));
typedef u32 u32x4 __attribute__((ext_vector_type(4)));
typedef float f32x2 __attribute__((ext_vector_type(2)));
typedef float f32x4 __attribute__((ext_vector_type(4)));
typedef float f32x16 __attribute__((ext_vector_type(16)));
typedef __bf16 bf16x2 __attribute__((ext_vector_type(2)));
typedef __bf16 bf16x8 __attribute__((ext_vector_type(8)));
typedef __amdgpu_buffer_rsrc_t rsrc_t;
#define DI __device__ __forceinline__
#define SB() __builtin_amdgcn_sched_barrier(0)

#define PROF_DECL
#define STAMP(i)
#define STAMPV(i)
#define CSTAMP(i)
#define PROF_END(kid)

namespace {
typedef unsigned long long u64;
constexpr int S = 2048, D = 640, F = 1728;
constexpr u32 ACT = S * D * 2;
constexpr u32 FSTR = 256, FARR = 8 * 256 * FSTR;
constexpr u32 OFF_EP = 0;
constexpr u32 OFF_FM = 4096, OFF_FS = OFF_FM + FARR, OFF_FW = OFF_FS + FARR;
constexpr u32 OFF_X = OFF_FW + FARR;
constexpr u32 OFF_T = OFF_X + 176 * 16384;
constexpr u32 OFF_WT = OFF_T + 176 * 16384;
constexpr u32 OFF_W1 = OFF_WT + 176 * 16384;
constexpr u32 OFF_W2 = OFF_W1 + 819200;
constexpr u32 OFF_W3 = OFF_W2 + 819200;
constexpr u32 OFF_W4 = OFF_W3 + 16 * 143360;
constexpr u32 OFF_H = OFF_W4 + 8 * 276480;
constexpr u32 OFF_G1 = OFF_H + ACT;
constexpr u32 OFF_X1 = OFF_G1 + ACT;
constexpr u32 OFF_G3 = OFF_X1 + ACT;
constexpr u32 OFF_S1 = OFF_G3 + S * F * 2;
constexpr u32 OFF_S2 = OFF_S1 + S * 16;
constexpr u32 OFF_CSP = OFF_S2 + 128 * 1024;
constexpr u32 NCS = 152 * 16;
constexpr u32 WS_USED = OFF_CSP + 20 * NCS * 4;

struct Tab { u32 v[256]; };
constexpr double c_sin0(double x) { double t = x, s = x; for (int i = 1; i < 10; ++i) { t *= -x * x / ((2 * i) * (2 * i + 1)); s += t; } return s; }
constexpr double c_cos0(double x) { double t = 1, s = 1; for (int i = 1; i < 10; ++i) { t *= -x * x / ((2 * i - 1) * (2 * i)); s += t; } return s; }
constexpr double c_cos(int n, int den) {
  n = (4 * n) % (4 * den);
  const int qd = n / den, r = n % den;
  const double x = 1.5707963267948966 * r / den;
  return qd == 0 ? c_cos0(x) : qd == 1 ? -c_sin0(x) : qd == 2 ? -c_cos0(x) : c_sin0(x);
}
constexpr double c_sin(int n, int den) {
  n = (4 * n) % (4 * den);
  const int qd = n / den, r = n % den;
  const double x = 1.5707963267948966 * r / den;
  return qd == 0 ? c_sin0(x) : qd == 1 ? c_cos0(x) : qd == 2 ? -c_sin0(x) : -c_cos0(x);
}
constexpr u32 c_f32(double x) { return __builtin_bit_cast(u32, (float)x); }
constexpr u32 c_bf(double x) { u32 u = c_f32(x); u += 0x7fffu + ((u >> 16) & 1u); return u >> 16; }
constexpr u32 c_pk(double lo, double hi) { return c_bf(lo) | (c_bf(hi) << 16); }
constexpr Tab make_tab(int id) {
  Tab T{};
  for (int l = 0; l < 64; ++l)
    for (int j = 0; j < 4; ++j) {
      const int m = l & 15, q = l >> 4;
      u32 v = 0;
      if (id < 2) {
        double e[2] = {0, 0};
        for (int h = 0; h < 2; ++h) { int jj = 2 * j + h; if (jj < 5) e[h] = id ? -c_sin((5 * q + jj) * m, 20) : c_cos((5 * q + jj) * m, 20); }
        v = c_pk(e[0], e[1]);
      } else if (id < 8) {
        const int k2 = 16 * (id & 1) + m, n = 8 * q + 2 * j;
        v = id < 4 ? c_pk(c_cos(k2 * n, 32), c_cos(k2 * (n + 1), 32))
                   : id < 6 ? c_pk(c_sin(k2 * n, 32), c_sin(k2 * (n + 1), 32)) : c_pk(-c_sin(k2 * n, 32), -c_sin(k2 * (n + 1), 32));
      } else if (id < 12) {
        const int a = (8 * q + 2 * j + (id & 1)) * m;
        v = c_f32(id < 10 ? c_cos(a, 640) : c_sin(a, 640));
      } else if (id < 16) {
        const int a = (4 * q + j) * m;
        v = id == 12 ? c_pk(c_cos(a, 16), c_sin(a, 16)) : id == 13 ? c_pk(-c_sin(a, 16), c_cos(a, 16)) : c_f32(id == 14 ? c_cos(a, 256) : c_sin(a, 256));
      } else if (id < 24) {
        const int t = (id >> 1) & 1, kap = id & 1, n2 = 8 * (m >> 2) + 4 * t + (m & 3), a = (16 * kap + 4 * q + j) * n2;
        v = id < 20 ? c_pk(c_cos(a, 32), c_sin(a, 32)) : c_pk(-c_sin(a, 32), c_cos(a, 32));
      } else if (id < 26) {
        const int k1 = 4 * q + j, n1 = 16 * (id & 1) + m;
        const double wg = (k1 == 0 || k1 == 10) ? 1. : (k1 < 10 ? 2. : 0.), sw = (k1 > 0 && k1 < 10) ? 2. : 0.;
        v = n1 < 20 ? c_pk(wg * c_cos(k1 * n1, 20), sw * c_sin(k1 * n1, 20)) : 0u;
      } else if (id < 30) {
        const int n2 = 8 * (m >> 2) + 4 * (id & 1) + (m & 3), a = (4 * q + j) * n2;
        v = c_f32((id < 28 ? c_cos(a, 640) : c_sin(a, 640)) / (2048. * 640.));
      } else {
        const int n2 = 8 * (m >> 2) + 4 * (id & 1) + (m & 3);
        v = (8 * q + 2 * j == n2 ? 0x3f80u : 0u) | (8 * q + 2 * j + 1 == n2 ? 0x3f800000u : 0u);
      }
      T.v[l * 4 + j] = v;
    }
  return T;
}
template <int I> constexpr Tab tab_c = make_tab(I);
__device__ const __attribute__((aligned(1024))) Tab g_tab[32] = {
    tab_c<0>, tab_c<1>, tab_c<2>, tab_c<3>, tab_c<4>, tab_c<5>, tab_c<6>, tab_c<7>, tab_c<8>, tab_c<9>, tab_c<10>,
    tab_c<11>, tab_c<12>, tab_c<13>, tab_c<14>, tab_c<15>, tab_c<16>, tab_c<17>, tab_c<18>, tab_c<19>, tab_c<20>, tab_c<21>,
    tab_c<22>, tab_c<23>, tab_c<24>, tab_c<25>, tab_c<26>, tab_c<27>, tab_c<28>, tab_c<29>, tab_c<30>, tab_c<31>};

template <int I, int N, class Fn> DI void sfor(Fn&& f) {
  if constexpr (I < N) { f(std::integral_constant<int, I>{}); sfor<I + 1, N>(f); }
}
DI float bflo(u32 v) { return __uint_as_float(v << 16); }
DI float bfhi(u32 v) { return __uint_as_float(v & 0xffff0000u); }
DI f32x2 up2(u32 v) { return f32x2{bflo(v), bfhi(v)}; }
DI u32 pk2(f32x2 v) { return __builtin_bit_cast(u32, __builtin_convertvector(v, bf16x2)); }
DI u32 pk(float lo, float hi) { return pk2(f32x2{lo, hi}); }
DI float rbf(float x) { return bflo(pk(x, x)); }
DI f32x2 fma2(f32x2 a, f32x2 b, f32x2 c) { return __builtin_elementwise_fma(a, b, c); }
DI f32x2 sp2(float a) { return f32x2{a, a}; }
DI f32x2 lo2(f32x4 a) { return f32x2{a[0], a[1]}; }
DI f32x2 hi2(f32x4 a) { return f32x2{a[2], a[3]}; }
DI f32x4 cat4(f32x2 a, f32x2 b) { return f32x4{a[0], a[1], b[0], b[1]}; }
DI float dot2(u32 a, u32 b, float c) { return __builtin_amdgcn_fdot2_f32_bf16(__builtin_bit_cast(bf16x2, a), __builtin_bit_cast(bf16x2, b), c, false); }
DI f32x4 mfma16(u32x4 a, u32x4 b, f32x4 c) {
  return __builtin_amdgcn_mfma_f32_16x16x32_bf16(__builtin_bit_cast(bf16x8, a), __builtin_bit_cast(bf16x8, b), c, 0, 0, 0);
}
DI f32x16 mfma32(u32x4 a, u32x4 b, f32x16 c) {
  return __builtin_amdgcn_mfma_f32_32x32x16_bf16(__builtin_bit_cast(bf16x8, a), __builtin_bit_cast(bf16x8, b), c, 0, 0, 0);
}
template <int CTRL> DI float dpp(float x) {
  return __builtin_bit_cast(float, __builtin_amdgcn_mov_dpp(__builtin_bit_cast(int, x), CTRL, 0xf, 0xf, true));
}
DI float row_sum(float x) {
  x += dpp<0xB1>(x);
  x += dpp<0x4E>(x);
  x += dpp<0x141>(x);
  x += dpp<0x140>(x);
  return x;
}
DI float fold32(float a, float b) {
  auto t = __builtin_amdgcn_permlane32_swap(__float_as_uint(a), __float_as_uint(b), false, false);
  return __uint_as_float(t[0]) + __uint_as_float(t[1]);
}
DI float fold16(float a, float b) {
  auto t = __builtin_amdgcn_permlane16_swap(__float_as_uint(a), __float_as_uint(b), false, false);
  return __uint_as_float(t[0]) + __uint_as_float(t[1]);
}
DI float rdl(float x, int lane) { return __uint_as_float(__builtin_amdgcn_readlane(__float_as_uint(x), lane)); }
DI float ln_std(float var) { return rbf(__builtin_amdgcn_sqrtf(rbf(rbf(var) + 1e-5f))); }
DI void twid(f32x4& re, f32x4& im, f32x4 c, f32x4 s) {
  f32x2 r0 = fma2(lo2(re), lo2(c), lo2(im) * lo2(s)), r1 = fma2(hi2(re), hi2(c), hi2(im) * hi2(s));
  f32x2 i0 = fma2(-lo2(re), lo2(s), lo2(im) * lo2(c)), i1 = fma2(-hi2(re), hi2(s), hi2(im) * hi2(c));
  re = cat4(r0, r1); im = cat4(i0, i1);
}
DI u32x4 pkc(f32x4 a, f32x4 b) { return u32x4{pk(a[0], b[0]), pk(a[1], b[1]), pk(a[2], b[2]), pk(a[3], b[3])}; }
typedef f32x4 Gelu;
DI Gelu gelu_consts(float s, float ga) {
  const float c0 = -2.f * 0.7978845608028654f * 1.4426950408889634f, c1 = c0 * 0.044715f, rs = __builtin_amdgcn_rcpf(s);
  return Gelu{c0 * rs, c1 * rs * rs * rs, s, ga};
}
DI f32x2 gelu2(f32x2 a, Gelu g) {
  f32x2 z = fma2(a * a, sp2(g[1]), sp2(g[0])) * a;
  f32x2 e = {__builtin_amdgcn_exp2f(z[0]), __builtin_amdgcn_exp2f(z[1])};
  f32x2 d = fma2(e, sp2(g[2]), sp2(g[2]));
  return a * f32x2{__builtin_amdgcn_rcpf(d[0]), __builtin_amdgcn_rcpf(d[1])};
}
DI f32x4 gelu4(f32x4 a, f32x4 cs, Gelu g) {
  return cat4(gelu2(fma2(lo2(cs), sp2(g[3]), lo2(a)), g), gelu2(fma2(hi2(cs), sp2(g[3]), hi2(a)), g));
}
DI u32 opq(u32 v) { asm volatile("" : "+v"(v)); return v; }
DI void tie(u32x4& a) { asm volatile("" : "+v"(a)); }
DI void tie(f32x4& a) { asm volatile("" : "+v"(a)); }
template <int N> DI void wait_vm() { asm volatile("s_waitcnt vmcnt(%0)" ::"i"(N) : "memory"); }
DI void bar() { asm volatile("s_waitcnt lgkmcnt(0)\n\ts_barrier" ::: "memory"); }
DI void bar_raw() { asm volatile("s_barrier" ::: "memory"); }
DI u32x4 ld1(rsrc_t r, u32 voff, u32 soff) { return __builtin_amdgcn_raw_buffer_load_b128(r, voff, soff, 16); }
DI u32x2 ld1h(rsrc_t r, u32 voff, u32 soff) { return __builtin_amdgcn_raw_buffer_load_b64(r, voff, soff, 16); }
DI void st1(u32x4 v, rsrc_t r, u32 voff, u32 soff) {
  __builtin_amdgcn_raw_buffer_store_b128(v, r, voff, soff, 16);
  asm volatile("s_nop 1" ::"v"(v) : "memory");
}
DI u32x4 lds4(const char* p) { return *(const u32x4*)p; }
DI f32x4 ldsf(const char* p) { return *(const f32x4*)p; }
#define GL "\n\tglobal_load_lds_dwordx4 %1, %2"
template <int N> DI void glds(u32 ldsaddr, const void* sbase, u32 voff) {
  if constexpr (N == 1) asm volatile("s_mov_b32 m0, %0\n\ts_nop 4" GL " sc1" ::"s"(ldsaddr), "v"(voff), "s"(sbase) : "memory");
  else if constexpr (N == 2) asm volatile("s_mov_b32 m0, %0\n\ts_nop 4" GL " sc1" GL " offset:1024 sc1" ::"s"(ldsaddr), "v"(voff), "s"(sbase) : "memory");
  else if constexpr (N == 3) asm volatile("s_mov_b32 m0, %0\n\ts_nop 4" GL " sc1" GL " offset:1024 sc1" GL " offset:2048 sc1" ::"s"(ldsaddr), "v"(voff), "s"(sbase) : "memory");
  else asm volatile("s_mov_b32 m0, %0\n\ts_nop 4" GL " sc1" GL " offset:1024 sc1" GL " offset:2048 sc1" GL " offset:3072 sc1" ::"s"(ldsaddr), "v"(voff), "s"(sbase) : "memory");
}
template <int N> DI void glds_n(u32 ldsaddr, const char* sbase, u32 voff) {
  sfor<0, (N + 3) / 4>([&](auto I) {
    constexpr int i = I.value, c = (N - 4 * i) < 4 ? (N - 4 * i) : 4;
    glds<c>(ldsaddr + i * 4096, sbase + i * 4096, voff);
  });
}
template <int NTOT> DI void copy4(u32 ldsaddr, const char* src, int hw, u32 voff) {
  constexpr int per = NTOT / 4, rem = NTOT % 4;
  if constexpr (rem) { if (hw < rem) glds<1>(ldsaddr + (4 * per + hw) * 1024, src + (4 * per + hw) * 1024, voff); }
  glds_n<per>(ldsaddr + hw * per * 1024, src + hw * per * 1024, voff);
}
template <int NTOT> DI void copy3(u32 ldsaddr, const char* src, int hw, u32 voff) {
  constexpr int per = NTOT / 3, rem = NTOT % 3;
  if constexpr (rem) { if (hw < rem) glds<1>(ldsaddr + (3 * per + hw) * 1024, src + (3 * per + hw) * 1024, voff); }
  glds_n<per>(ldsaddr + hw * per * 1024, src + hw * per * 1024, voff);
}
template <int N> DI void touch(u32 dummy, const char* p, int l) {
  if (N > 1 || l == 12345) return;
#pragma unroll
  for (int i = 0; i < N; ++i)
    asm volatile("s_mov_b32 m0, %0\n\ts_nop 4\n\tglobal_load_lds_dword %1, %2 sc1" ::"s"(dummy), "v"(l * 128), "s"(p + i * 8192) : "memory");
}
DI int rot5(int cb) { return (cb * 5) >> 1; }
DI int rot3(int cb) { return (cb * 5) >> 2; }
DI int rot4(int cb) { return (cb * 27) >> 2; }

DI void pre_load(int t, const u16* w1, const u16* w2, const u16* w3, const u16* w4, int l, u32 (&v)[16]) {
  const u16* W; int N = D, ks, nb;
  if (t < 400) { int m2 = t >= 200; t -= 200 * m2; W = m2 ? w2 : w1; ks = t / 10; nb = t - 10 * ks; }
  else if (t < 940) { t -= 400; W = w3; N = F; ks = t / 27; nb = t - 27 * ks; }
  else { t -= 940; W = w4; ks = t / 10; nb = t - 10 * ks; }
  const char* src = (const char*)(W + (size_t)(ks * 32) * N + nb * 64);
  const u32 vo = (l >> 5) * 16 * N + (l & 31) * 4;
#pragma unroll
  for (int j = 0; j < 16; ++j) v[j] = *(const u32*)(src + (size_t)((16 * (j >> 3) + (j & 7)) * N * 2) + vo);
}
DI void pre_store(int t, rsrc_t rs, int l, const u32 (&v)[16]) {
  int ks, nb, NT = 5, KS = 20, mat; u32 dst;
  if (t < 400) { mat = t >= 200; t -= 200 * mat; ks = t / 10; nb = t - 10 * ks; dst = mat ? OFF_W2 : OFF_W1; }
  else if (t < 940) { mat = 2; t -= 400; ks = t / 27; nb = t - 27 * ks; NT = 7; dst = OFF_W3; }
  else { mat = 3; t -= 940; ks = t / 10; nb = t - 10 * ks; dst = OFF_W4; KS = 54; }
  const int c32 = l & 31, hq = l >> 5, c = l & 15;
  u32 tb[4], ct[4]; bool solo[2];
#pragma unroll
  for (int i = 0; i < 4; ++i) {
    const int g = 4 * nb + i, cb = NT == 5 ? g / 5 : g / 7, pos = g - cb * NT;
    const int li = (cb & 1) ? (pos == 0 ? NT - 1 : pos - 1) : pos;
    int kp = ks - (mat < 2 ? rot5(cb) : mat == 2 ? rot3(cb) : rot4(cb));
    if (kp < 0) kp += KS;
    tb[i] = dst + ((cb * KS + kp) * NT + li) * 1024;
    ct[i] = (mat * 20 + cb * NT + li) * 64;
    if (!(i & 1)) solo[i >> 1] = li == NT - 1;
  }
  const bool hb = c32 >= 16, so = hb ? solo[1] : solo[0];
  const u32 t0 = hb ? tb[2] : tb[0], t1 = hb ? tb[3] : tb[1];
  const u32 ts = (c >= 8 ? t1 : t0) + (c & 7) * 32;
  const u32 o0 = (so ? ts : t0 + c * 16) + hq * 256, o1 = (so ? ts + 16 : t1 + c * 16) + hq * 256;
  float cs[2] = {0.f, 0.f};
#pragma unroll
  for (int p = 0; p < 2; ++p)
#pragma unroll
    for (int t2 = 0; t2 < 2; ++t2) {
      u32x4 pc;
#pragma unroll
      for (int jj = 0; jj < 4; ++jj) pc[jj] = __builtin_amdgcn_perm(v[8 * p + 2 * jj + 1], v[8 * p + 2 * jj], t2 ? 0x07060302u : 0x05040100u);
      st1(pc, rs, (t2 ? o1 : o0) + p * 512, 0);
#pragma unroll
      for (int jj = 0; jj < 4; ++jj) cs[t2] = dot2(pc[jj], 0x3f803f80u, cs[t2]);
    }
  if (!(mat & 1)) {
    const float r = fold32(cs[0], cs[1]);
    const u32 c0 = hb ? ct[2] : ct[0], c1 = hb ? ct[3] : ct[1];
    const u32 o = so ? (c >= 8 ? c1 : c0) + (2 * (c & 7) + hq) * 4 : (hq ? c1 : c0) + c * 4;
    __builtin_amdgcn_raw_buffer_store_b32(__float_as_uint(r), rs, o, OFF_CSP + ks * NCS * 4, 16);
  }
}

template <bool FWD> DI u32x4 radix8_const(int l, int s2) {
  const int i = l & 31, h = l >> 5;
  const int o = (i & 4) + ((i >> 1) & 1) + ((i >> 2) & 2), c = i & 1;
  const float fo = (float)o;
  const float a0 = FWD ? fo * (float)(1024 * h + s2) * (1.f / 2048.f) : (float)(4 * h) * (fo * 256.f + (float)s2) * (1.f / 2048.f);
  const float da = FWD ? fo * 0.125f : (fo * 256.f + (float)s2) * (1.f / 2048.f);
  u32x4 r;
#pragma unroll
  for (int jj = 0; jj < 4; ++jj) {
    float a = a0 + (c ? 0.25f : 0.f) + jj * da;
    u32 v = pk(__builtin_amdgcn_cosf(a), __builtin_amdgcn_sinf(a));
    r[jj] = (i < 16) ? v : 0u;
  }
  return r;
}

constexpr int K1_TS = 1040, K3_KS = 144, K3_RS = 11 * 144;
constexpr int L_A = 102400;
constexpr int L_XR = 113856;
constexpr int L_B = 124096;
constexpr int L_W2 = 60416 - 256, L_DUMMY = 161536, L_MISC = 161792, L_TOT = 163840;
constexpr int L_CNT = L_MISC + 768, L_ZERO = L_MISC + 816, L_ST = L_MISC + 1024;
static_assert(L_A + 11 * K1_TS <= L_XR && L_XR + 10240 <= L_B && L_B + 20480 <= L_W2 - 1024 + 88064 && L_A + 20 * 528 <= L_W2 - 1024 + 57344 &&
              L_W2 - 1024 + 102400 <= L_DUMMY && 57344 <= L_W2 - 1024, "lds map");
constexpr int SPIN_MAX = 1 << 16;
constexpr int CODE_KB = 48;

DI void arrive(char* lds, char* flag, int slot, u32 nw, u64 val, int l) {
  wait_vm<0>();
  u32 old = 0;
  if (l == 0) old = __hip_atomic_fetch_add((u32*)(lds + L_CNT + slot * 4), 1u, __ATOMIC_RELAXED, __HIP_MEMORY_SCOPE_WORKGROUP);
  if (__builtin_amdgcn_readfirstlane(old) == nw - 1 && l < 8)
    __hip_atomic_store((u64*)(flag + l * (256 * FSTR)), val, __ATOMIC_RELAXED, __HIP_MEMORY_SCOPE_AGENT);
}
DI void poll(const char* rep, int first, int n, u64 want, int l) {
  if (l < n) {
    const u64* p = (const u64*)(rep + ((first + l) & 255) * FSTR);
    for (int s = 0; s < SPIN_MAX; ++s)
      if (__hip_atomic_load(p, __ATOMIC_RELAXED, __HIP_MEMORY_SCOPE_AGENT) - want < 8ull) break;
  }
  asm volatile("" ::: "memory");
}
DI void poll4(const char* rep, u64 want, int l) {
  const u64* p = (const u64*)(rep + l * FSTR);
  for (int s = 0; s < SPIN_MAX; ++s) {
    bool ok = true;
#pragma unroll
    for (int i = 0; i < 4; ++i) ok &= __hip_atomic_load(p + i * (64 * FSTR / 8), __ATOMIC_RELAXED, __HIP_MEMORY_SCOPE_AGENT) - want < 8ull;
    if (ok) break;
  }
  asm volatile("" ::: "memory");
}
DI u32x4 solo_merge(u32 d0, u32 d1) {
  auto a = __builtin_amdgcn_permlane16_swap(d0, d0, false, false);
  auto b = __builtin_amdgcn_permlane16_swap(d1, d1, false, false);
  return u32x4{a[0], b[0], a[1], b[1]};
}
template <int R0> constexpr int g12_ord(int j) { return (j & 3) == 3 ? R0 + (j >> 2) : ((j - (j >> 2)) + (R0 == 0 ? 5 : 0)); }
template <int R0, class LdFn> DI void gemm12(f32x4 (&acc)[5], const u32x4 (&wr)[25], const char* p, LdFn load) {
  constexpr int W = 8;
  u32x4 act[W], wf[3][5];
  sfor<0, W>([&](auto JI) { act[JI.value] = load(g12_ord<R0>(JI.value)); SB(); });
#pragma unroll
  for (int i = 0; i < 5; ++i) wf[0][i] = lds4(p + g12_ord<R0>(0) * 5120 + i * 1024);
#pragma unroll
  for (int i = 0; i < 5; ++i) wf[1][i] = lds4(p + g12_ord<R0>(1) * 5120 + i * 1024);
  SB();
  sfor<0, 20>([&](auto JI) {
    constexpr int j = JI.value, n = j - (j >> 2);
    sfor<0, 5>([&](auto II) {
      constexpr int i = II.value;
      if constexpr ((j & 3) == 3) acc[i] = mfma16(wr[(j >> 2) * 5 + i], act[j % W], acc[i]);
      else acc[i] = mfma16(wf[n % 3][i], act[j % W], acc[i]);
      SB();
      constexpr int slot = j * 5 + i;
      if constexpr ((slot & 3) != 3) {
        constexpr int f = 10 + slot - (slot >> 2), fn = f / 5, fi = f % 5;
        if constexpr (f < 75) { constexpr int kj = fn + fn / 3; wf[fn % 3][fi] = lds4(p + g12_ord<R0>(kj) * 5120 + fi * 1024); }
      }
      if constexpr (i == 1 && j >= 1 && j - 1 + W < 20) act[(j - 1) % W] = load(g12_ord<R0>(j - 1 + W));
      SB();
    });
  });
}
template <int N> DI void wait_lgkm() { __builtin_amdgcn_s_waitcnt(0xC07F | (N << 8)); }
template <int NK, int NT, int W, class OffFn, class LdFn, class HookFn>
DI void gemm32(f32x4 (&acc)[2][NT], const char* lds, u32 p0, OffFn off, LdFn load, HookFn hook) {
  u32x4 act[W][2], wf[2][NT];
  sfor<0, W>([&](auto KI) { act[KI.value][0] = load(KI, 0); SB(); act[KI.value][1] = load(KI, 1); SB(); });
  const char* pb[3] = {lds + opq(p0), lds + opq(p0 + 61440), lds + opq(p0 + 122880)};
#pragma unroll
  for (int i = 0; i < NT; ++i) wf[0][i] = lds4(pb[0] + off(0) + i * 1024);
  SB();
  sfor<0, NK>([&](auto KI) {
    constexpr int k = KI.value;
    wait_lgkm<0>();
    sfor<0, 2 * NT>([&](auto II) {
      constexpr int n = II.value, i = n >> 1, j = n & 1;
      acc[j][i] = mfma16(wf[k & 1][i], act[k % W][j], acc[j][i]);
      SB();
      if constexpr (n < NT && k + 1 < NK) { constexpr int o = off(k + 1) + n * 1024; wf[(k + 1) & 1][n] = lds4(pb[o / 61440] + o % 61440); }
      if constexpr ((n == NT || n == NT + 2) && k >= 1 && k - 1 + W < NK) act[(k - 1) % W][(n - NT) >> 1] = load(std::integral_constant<int, k - 1 + W>{}, (n - NT) >> 1);
      SB();
    });
    hook(KI);
    SB();
  });
}

__global__ __launch_bounds__(512, 2) void fused_block(const u16* __restrict__ x, const u16* __restrict__ wr, const u16* __restrict__ wi,
                                                      const u16* __restrict__ w1, const u16* __restrict__ w2, const u16* __restrict__ w3,
                                                      const u16* __restrict__ w4, char* __restrict__ ws, u16* __restrict__ out) {
  extern __shared__ __attribute__((aligned(16))) char lds[];
  const char* pc0;
  asm volatile("s_getpc_b64 %0" : "=s"(pc0));
  PROF_DECL; STAMP(0);
  const int b = blockIdx.x, xcd = b & 7, i5 = b >> 3;
  const int tid = threadIdx.x, w = __builtin_amdgcn_readfirstlane(tid >> 6), l = tid & 63;
  const int s2 = xcd * 32 + i5, tb = xcd * 4 + (i5 >> 3), cb = i5 & 7, tb3 = xcd * 2 + (i5 >> 4), cb3 = i5 & 15;
  const int hw = w & 3, g = cb >> 1;
  const u32 lb = (u32)(size_t)lds, l16 = l * 16;
  const rsrc_t rs = __builtin_amdgcn_make_buffer_rsrc(ws, 0, WS_USED, 0x00020000);
  const u64 ep = __hip_atomic_load((const u64*)(ws + OFF_EP), __ATOMIC_RELAXED, __HIP_MEMORY_SCOPE_AGENT);
  char* fl = ws + s2 * FSTR;
  const char* rp = ws + xcd * (256 * FSTR);
  const int pfirst = 8 * tb - ((g & 1) ? 0 : 1 + (g >> 1)), pn = 9 + (g >> 1);
  if (w >= 4) {
    const char* w1p = ws + OFF_W1 + cb * 102400;
    const char* w2p = ws + OFF_W2 + cb * 102400;
    const char* w3p = ws + OFF_W3 + cb3 * 143360;
    const char* w4p = ws + OFF_W4 + cb * 276480;
    const int half = b & 1, cq = (b >> 1) & 3, qq = (b >> 3) & 3, u1 = b >> 5;
#pragma unroll
    for (int n = 0; n < 5; ++n) {
      const int c = n * 64 + l, rm = c / 10, cc = c - 10 * rm;
      const int u = u1 + 8 * (4 * cq + (rm >> 3) + 16 * (4 * qq + ((rm >> 1) & 3)));
      const int uu = half ? ((S - u) & (S - 1)) : u;
      const char* src = (const char*)((rm & 1) ? wi : wr) + ((uu * 642 + (half ? 484 - 160 * hw : 160 * hw)) & ~3) + cc * 16;
      asm volatile("s_mov_b32 m0, %0\n\ts_nop 4\n\tglobal_load_lds_dwordx4 %1, off" ::"s"(lb + L_B + hw * 5120 + n * 1024), "v"(src) : "memory");
    }
    const int hid = hw * 256 + b, ta = hid < 400 ? hid : hid < 456 ? hid + 1024 : -1, tc = 400 + hid;
    const u32 dm = lb + L_DUMMY;
    u32 va[16], vc[16];
    if (ta >= 0) {
      pre_load(ta, w1, w2, w3, w4, l, va);
      wait_vm<16>();
    } else {
      if (i5 < 5) {
        const int n = i5 * 2 + (hw & 1);
        if (n < CODE_KB / 8) touch<1>(dm, pc0 + n * 8192, l);
        else touch<1>(dm, (const char*)g_tab + (n - CODE_KB / 8) * 8192, l);
        wait_vm<1>();
      } else {
        wait_vm<0>();
      }
    }
    {
      const int base = half ? ((u1 & 1) ? 482 : 484) - 160 * hw : 160 * hw - ((u1 & 1) ? 2 : 0);
#pragma unroll
      for (int n = 0; n < 3; ++n) {
        const int it = n * 64 + l;
        if (it < 176) {
          const int ci = it & 3, cl = it >> 2, k2l = cl / 11, k1 = cl - 11 * k2l, k2 = 16 * half + 4 * hw + k2l, k = k1 + 20 * k2;
          const char* p = lds + L_B + hw * 5120 + ci * 1280 + 2 * (half ? 640 - k : k) - base;
          u32x4 o;
#pragma unroll
          for (int r = 0; r < 4; ++r)
            o[r] = ((u32) * (const u16*)(p + r * 320) | ((u32) * (const u16*)(p + r * 320 + 160) << 16)) ^ (half ? 0x80000000u : 0u);
          st1(o, rs, OFF_WT + ((k1 * 8 + (k2 >> 2)) * 2 + (u1 >> 2)) * 16384 + ((k2 & 3) * 4 + (u1 & 3)) * 1024 + (qq * 16 + cq * 4 + ci) * 16, 0);
        }
      }
    }
    const u64 e = (ep + 1) << 4;
    STAMP(1);
    bar();
    arrive(lds, fl + OFF_FM, 0, 8, e + 1, l);
    STAMP(2);
    if (hid < 400) pre_store(ta, rs, l, va);
    arrive(lds, fl + OFF_FW, 1, 4, e + 1, l);
    pre_load(tc, w1, w2, w3, w4, l, vc);
    wait_vm<16>();
    STAMP(3);
    bar();
    bar();
    if (ta >= 400) pre_store(ta, rs, l, va);
    pre_store(tc, rs, l, vc);
    STAMP(4);
    bar();
    poll4(rp + OFF_FW, e + 1, l);
    arrive(lds, fl + OFF_FW, 8, 4, e + 2, l);
    STAMP(5);
    copy4<100>(lb, w1p, hw, l16);
    STAMP(6);
    bar();
    touch<1>(dm, ws + OFF_W2 + i5 * 25600 + hw * 6400, l < 50 ? l : 0);
    bar();
    copy4<14>(lb + L_W2 - 1024 + 88064, w2p + 88064, hw, l16);
    wait_vm<4>();
    STAMP(7);
    bar();
    copy4<30>(lb + L_W2 - 1024 + 57344, w2p + 57344, hw, l16);
    bar();
    poll4(rp + OFF_FW, e + 2, l);
    if (hw == 3) {
      touch<8>(dm, ws + OFF_W3 + i5 * 71680, l);
      touch<1>(dm, ws + OFF_W3 + i5 * 71680 + 65536, l < 48 ? l : 0);
      bar();
      bar();
      touch<8>(dm, ws + OFF_W4 + i5 * 69120, l);
      touch<1>(dm, ws + OFF_W4 + i5 * 69120 + 65536, l < 28 ? l : 0);
      bar();
      bar();
    } else {
      {
        const int c = hw * 64 + l;
        const u32 vo = (c < 80 ? cb * 80 + c : 640 + cb3 * 112 + c - 80) * 4;
        float a[20];
#pragma unroll
        for (int i = 0; i < 20; ++i) a[i] = __uint_as_float(__builtin_amdgcn_raw_buffer_load_b32(rs, vo, OFF_CSP + i * NCS * 4, 16));
        float t = 0.f;
#pragma unroll
        for (int i = 0; i < 20; ++i) t += a[i];
        *(float*)(lds + L_MISC + c * 4) = t;
      }
      if (hw == 2) {
        poll(rp + OFF_FS, pfirst, pn, e + 1, l);
        const int rho = tb * 64 + l, shift = (g & 1) ? -(1 + (g >> 1)) : (1 + (g >> 1));
        const int sp = (rho & 7) * 256 + (rho >> 3) - shift;
        const bool ok = sp >= 0 && sp < S;
        u32x2 ab = ld1h(rs, ok ? ((sp & 255) * 8 + (sp >> 8)) * 16 : 0x80000000u, OFF_S1);
        const u32x2 mr = ld1h(rs, rho * 16 + 8, OFF_S1);
        *(u32x4*)(lds + L_ST + l16) = u32x4{ok ? ab[0] : 0x3f800000u, ab[1], mr[0], mr[1]};
      }
      STAMP(8);
      bar();
      copy3<56>(lb + L_W2 - 1024, w2p, hw, l16);
      copy3<56>(lb, w3p, hw, l16);
      wait_vm<18>();
      STAMP(9);
      bar();
      bar();
      copy3<84>(lb + 57344, w3p + 57344, hw, l16);
      wait_vm<0>();
      STAMP(13);
      bar();
    }
    u32x4 wk[30];
    const u32 wko = OFF_W4 + cb * 276480 + 153600 + hw * 10240;
    copy4<10>(lb + 143360, w4p + 143360, hw, l16);
#pragma unroll
    for (int i = 0; i < 10; ++i) wk[i] = ld1(rs, l16, wko + i * 1024);
    bar();
    copy4<35>(lb, w4p, hw, l16);
    bar();
    copy4<35>(lb + 35840, w4p + 35840, hw, l16);
    bar();
    copy4<35>(lb + 71680, w4p + 71680, hw, l16);
    bar();
    STAMP(15);
    copy4<35>(lb + 107520, w4p + 107520, hw, l16);
#pragma unroll
    for (int i = 10; i < 30; ++i) wk[i] = ld1(rs, l16, wko + (i / 10) * 40960 + (i % 10) * 1024);
    wait_vm<20>();
    STAMP(18);
    bar();
    char* wkd = lds + hw * 10240 + l16;
#pragma unroll
    for (int n = 0; n < 3; ++n) {
      bar();
#pragma unroll
      for (int i = 0; i < 10; ++i) *(u32x4*)(wkd + n * 40960 + i * 1024) = wk[n * 10 + i];
    }
    bar();
    bar();
    STAMP(22); PROF_END(0);
    return;
  }
  __builtin_amdgcn_s_setprio(1);
  const f32x4 z4 = {0, 0, 0, 0};
  const char* tabp = (const char*)g_tab + l16;
  u64 e;
  {
    const int m = l & 15, q = l >> 4;
    glds<1>(lb + L_XR + w * 2560, x + (2 * w * 256 + s2) * D, l16);
    glds<1>(lb + L_XR + w * 2560 + 1024, x + (2 * w * 256 + s2) * D, l < 16 ? 1024 + l16 : 256 * D * 2 - 256 + l16);
    if (l < 32) glds<1>(lb + L_XR + w * 2560 + 2048, x + ((2 * w + 1) * 256 + s2) * D, 768 + l16);
    SB();
    u32x4 tbl[12];
#pragma unroll
    for (int i = 0; i < 12; ++i) tbl[i] = *(const u32x4*)(tabp + i * 1024);
    SB();
    if (tid < 16) *(u32*)(lds + L_CNT + tid * 4) = 0u;
    u32x4 r8 = radix8_const<true>(l, s2);
    tie(r8);
    u32 xrd = L_XR + w * 2560 + q * 320 + m * 4, ywr = L_A + m * K1_TS + (w >> 1) * 512 + q * 64 + (w & 1) * 8;
    u32 xo = OFF_X + (((l & 31) >> 2) * 2 + (l >> 5)) * 16384 + s2 * 64 + (l & 3) * 16;
    asm volatile("" : "+v"(xrd), "+v"(ywr), "+v"(xo));
    STAMP(1);
    wait_vm<12>();
    STAMP(2);
    u32 xv[2][5];
#pragma unroll
    for (int rr = 0; rr < 2; ++rr)
#pragma unroll
      for (int j = 0; j < 5; ++j) xv[rr][j] = *(const u32*)(lds + xrd + rr * 1280 + j * 64);
    float sm[2] = {0.f, 0.f}, sq[2] = {0.f, 0.f};
#pragma unroll
    for (int j = 0; j < 5; ++j)
#pragma unroll
      for (int rr = 0; rr < 2; ++rr) { sm[rr] = dot2(xv[rr][j], 0x3f803f80u, sm[rr]); sq[rr] = dot2(xv[rr][j], xv[rr][j], sq[rr]); }
    float red = row_sum(fold16(fold32(sm[0], sq[0]), fold32(sm[1], sq[1]))) * (1.f / D);
    auto sw = __builtin_amdgcn_permlane32_swap(__float_as_uint(red), __float_as_uint(red), false, false);
    const float mean = __uint_as_float(sw[0]);
    const float rsv = __builtin_amdgcn_rcpf(ln_std(fmaxf(__uint_as_float(sw[1]) - mean * mean, 0.f))), nbv = -rbf(mean) * rsv;
    u32x4 yo[2][2];
    f32x4 pr[2][2], pi[2][2];
#pragma unroll
    for (int rr = 0; rr < 2; ++rr) {
      const float rs_ = rdl(rsv, 16 * rr), nb = rdl(nbv, 16 * rr);
      f32x2 v[5];
#pragma unroll
      for (int j = 0; j < 5; ++j) v[j] = fma2(up2(xv[rr][j]), sp2(rs_), sp2(nb));
#pragma unroll
      for (int t = 0; t < 2; ++t) {
        const u32x4 a = {pk(v[0][t], v[1][t]), pk(v[2][t], v[3][t]), pk(v[4][t], 0.f), 0u};
        pr[rr][t] = mfma16(a, tbl[0], z4); pi[rr][t] = mfma16(a, tbl[1], z4);
      }
    }
#pragma unroll
    for (int rr = 0; rr < 2; ++rr) {
#pragma unroll
      for (int t = 0; t < 2; ++t) twid(pr[rr][t], pi[rr][t], __builtin_bit_cast(f32x4, tbl[8 + t]), __builtin_bit_cast(f32x4, tbl[10 + t]));
      const u32x4 br = pkc(pr[rr][0], pr[rr][1]), bi = pkc(pi[rr][0], pi[rr][1]);
#pragma unroll
      for (int t = 0; t < 2; ++t) {
        pr[rr][t] = mfma16(tbl[4 + t], bi, mfma16(tbl[2 + t], br, z4));
        pi[rr][t] = mfma16(tbl[6 + t], br, mfma16(tbl[2 + t], bi, z4));
      }
    }
#pragma unroll
    for (int rr = 0; rr < 2; ++rr)
#pragma unroll
      for (int t = 0; t < 2; ++t) yo[rr][t] = pkc(pr[rr][t], pi[rr][t]);
    if (m < 11) {
#pragma unroll
      for (int t = 0; t < 2; ++t)
#pragma unroll
        for (int r = 0; r < 4; ++r) *(u32x2*)(lds + ywr + t * 256 + r * 16) = u32x2{yo[0][t][r], yo[1][t][r]};
    }
    e = (ep + 1) << 4;
    STAMP(3);
    bar();
    f32x16 z16;
#pragma unroll
    for (int i = 0; i < 16; ++i) z16[i] = 0.f;
    u32x4 bb[3];
    const int k3 = w < 3 ? w + 8 : 10;
#pragma unroll
    for (int n = 0; n < 3; ++n) bb[n] = lds4(lds + L_A + (n < 2 ? w + 4 * n : k3) * K1_TS + l16);
    f32x16 o[3];
#pragma unroll
    for (int n = 0; n < 3; ++n) o[n] = mfma32(r8, bb[n], z16);
    SB();
#pragma unroll
    for (int n = 0; n < 3; ++n)
      if (n < 2 || w < 3)
        st1(u32x4{pk(o[n][0], o[n][1]), pk(o[n][2], o[n][3]), pk(o[n][4], o[n][5]), pk(o[n][6], o[n][7])}, rs, xo, (w + 4 * n) * 262144);
    STAMP(4);
    arrive(lds, fl + OFF_FM, 0, 8, e + 1, l);
    STAMP(5);
  }
  {
    const int l = opq(l16) >> 4, m = l & 15, q = l >> 4;
    u32x4 tbl[4];
#pragma unroll
    for (int i = 0; i < 4; ++i) { tbl[i] = *(const u32x4*)(tabp + 12288 + i * 1024); tie(tbl[i]); }
    u32 goff = (l >> 2) * 64 + (((l & 3) ^ (l >> 4)) * 16), frd = L_B + (64 * q + m) * 64 + ((w ^ (m >> 2)) * 16);
    asm volatile("" : "+v"(goff), "+v"(frd));
    poll(rp + OFF_FM, w * 64, 64, e + 1, l);
    bar();
    STAMP(6);
    if (b == 0 && tid == 0) __hip_atomic_store((u64*)(ws + OFF_EP), ep + 1, __ATOMIC_RELAXED, __HIP_MEMORY_SCOPE_AGENT);
    if (b < 176) {
      u32x4 in[4], wv[4];
#pragma unroll
      for (int i = 0; i < 4; ++i) in[i] = ld1(rs, goff, OFF_X + b * 16384 + w * 4096 + i * 1024);
      SB();
#pragma unroll
      for (int i = 0; i < 4; ++i) wv[i] = ld1(rs, l16, OFF_WT + b * 16384 + w * 4096 + i * 1024);
      SB();
#pragma unroll
      for (int i = 0; i < 4; ++i) *(u32x4*)(lds + L_B + w * 4096 + i * 1024 + l16) = in[i];
      bar();
      STAMP(7);
#pragma unroll
      for (int i = 0; i < 4; ++i) in[i] = lds4(lds + frd + i * 1024);
      const u32x4 fre = tbl[0], fim = tbl[1];
      const f32x4 twc = __builtin_bit_cast(f32x4, tbl[2]), tws = __builtin_bit_cast(f32x4, tbl[3]);
      u32x4 a[4];
      f32x4 pr[4], pi[4];
#pragma unroll
      for (int t = 0; t < 4; ++t) {
        a[t] = u32x4{in[0][t], in[1][t], in[2][t], in[3][t]};
        pr[t] = mfma16(a[t], fre, z4); pi[t] = mfma16(a[t], fim, z4);
      }
#pragma unroll
      for (int t = 0; t < 4; ++t) {
        twid(pr[t], pi[t], twc, tws);
        a[t] = pkc(pr[t], pi[t]);
        pr[t] = mfma16(fre, a[t], z4); pi[t] = mfma16(fim, a[t], z4);
      }
#pragma unroll
      for (int t = 0; t < 4; ++t) {
        f32x4 ni = -pi[t], wre, wim;
#pragma unroll
        for (int r = 0; r < 4; ++r) { wre[r] = bflo(wv[t][r]); wim[r] = bfhi(wv[t][r]); }
        twid(pr[t], ni, wre, wim);
        a[t] = pkc(pr[t], ni);
        pr[t] = mfma16(a[t], fre, z4); pi[t] = mfma16(a[t], fim, z4);
      }
#pragma unroll
      for (int t = 0; t < 4; ++t) {
        twid(pr[t], pi[t], twc, tws);
        a[t] = pkc(pr[t], pi[t]);
        pr[t] = mfma16(fre, a[t], z4); pi[t] = mfma16(fim, a[t], z4);
      }
#pragma unroll
      for (int i = 0; i < 4; ++i)
        *(u32x4*)(lds + frd + i * 1024) = u32x4{pk(pr[0][i], pi[0][i]), pk(pr[1][i], pi[1][i]), pk(pr[2][i], pi[2][i]), pk(pr[3][i], pi[3][i])};
      bar();
      STAMP(8);
#pragma unroll
      for (int i = 0; i < 4; ++i) in[i] = lds4(lds + L_B + w * 4096 + i * 1024 + l16);
#pragma unroll
      for (int i = 0; i < 4; ++i) st1(in[i], rs, goff, OFF_T + b * 16384 + w * 4096 + i * 1024);
    } else {
      bar();
      bar();
    }
    arrive(lds, fl + OFF_FM, 2, 4, e + 2, l);
    STAMP(9);
  }
  const int T = tb * 4 + w;
  const int kbp = g * 5 + ((cb & 1) ? 3 : 0);
  u32x4 wr_[25];
  {
    const int l = opq(l16) >> 4, m = l & 15, q = l >> 4;
    u32x4 tbl[16];
#pragma unroll
    for (int i = 0; i < 16; ++i) tbl[i] = *(const u32x4*)(tabp + 16384 + i * 1024);
    u32x4 r8 = radix8_const<false>(l, s2);
    tie(r8);
    f32x4 y[2][2][2];
    {
      const u32 xrd0 = L_XR + 2 * w * 1280 + m * 64 + q * 16;
#pragma unroll
      for (int rr = 0; rr < 2; ++rr) {
        const u32x4 x0 = lds4(lds + xrd0 + rr * 1280), x1 = lds4(lds + (m < 4 ? xrd0 + rr * 1280 + 1024 : L_ZERO));
#pragma unroll
        for (int t = 0; t < 2; ++t) { y[rr][0][t] = mfma16(tbl[14 + t], x0, z4); y[rr][1][t] = mfma16(tbl[14 + t], x1, z4); tie(y[rr][0][t]); tie(y[rr][1][t]); }
      }
    }
#pragma unroll
    for (int i = 0; i < 14; ++i) tie(tbl[i]);
    u32 zo = OFF_T + s2 * 64 + (((l & 31) >> 2) * 2 + (l >> 5)) * 16384 + (l & 3) * 16;
    u32 zrd = L_B + 2 * w * K3_RS + (m < 11 ? m : 10) * K3_KS + q * 16;
    u32 hwr = L_A + m * 528 + q * 128 + w * 32;
    u32 hrd = L_A + (l >> 5) * 528 + (l & 31) * 16, hgo = (l >> 3) * 256 + (l & 7) * 16;
    u32 zwr = L_B + 4 * (l >> 5) * K3_RS + (l & 31) * 4 + w * K3_KS, zwr2 = zwr + (w < 3 ? 8 : 7) * K3_KS;
    asm volatile("" : "+v"(zrd), "+v"(zo), "+v"(hwr), "+v"(hrd), "+v"(hgo), "+v"(zwr), "+v"(zwr2));
    if ((l & 31) < 22) poll(rp + OFF_FM, w * 64, 64, e + 2, l);
    bar();
    STAMP(10);
    u32x4 zb[3];
    zb[0] = ld1(rs, zo, w * 262144);
    zb[1] = ld1(rs, zo, (w + 4) * 262144);
    zb[2] = ld1(rs, zo, (w < 3 ? w + 8 : 10) * 262144);
    f32x16 z16;
#pragma unroll
    for (int i = 0; i < 16; ++i) z16[i] = 0.f;
    f32x16 o[3];
#pragma unroll
    for (int n = 0; n < 3; ++n) o[n] = mfma32(r8, zb[n], z16);
    SB();
#pragma unroll
    for (int n = 0; n < 3; ++n)
#pragma unroll
      for (int i = 0; i < 4; ++i) *(u32*)(lds + (n < 2 ? zwr + 4 * n * K3_KS : zwr2) + i * K3_RS) = pk(o[n][2 * i], o[n][2 * i + 1]);
    bar();
    STAMP(11);
    u32x4 za[2][2];
#pragma unroll
    for (int rr = 0; rr < 2; ++rr) { za[rr][0] = lds4(lds + zrd + rr * K3_RS); za[rr][1] = lds4(lds + zrd + rr * K3_RS + 64); }
    SB();
    f32x4 qr[2][2], qi[2][2];
#pragma unroll
    for (int t = 0; t < 2; ++t)
#pragma unroll
      for (int rr = 0; rr < 2; ++rr) {
        qr[rr][t] = mfma16(za[rr][1], tbl[2 * t + 1], mfma16(za[rr][0], tbl[2 * t], z4));
        qi[rr][t] = mfma16(za[rr][1], tbl[2 * t + 5], mfma16(za[rr][0], tbl[2 * t + 4], z4));
      }
#pragma unroll
    for (int t = 0; t < 2; ++t)
#pragma unroll
      for (int rr = 0; rr < 2; ++rr) {
        twid(qr[rr][t], qi[rr][t], __builtin_bit_cast(f32x4, tbl[10 + t]), __builtin_bit_cast(f32x4, tbl[12 + t]));
        const u32x4 a = pkc(qr[rr][t], qi[rr][t]);
        y[rr][0][t] = mfma16(a, tbl[8], y[rr][0][t]); y[rr][1][t] = mfma16(a, tbl[9], y[rr][1][t]);
      }
    u32x4 hp[2][2];
#pragma unroll
    for (int rr = 0; rr < 2; ++rr)
#pragma unroll
      for (int n = 0; n < 2; ++n) hp[rr][n] = u32x4{pk2(lo2(y[rr][n][0])), pk2(hi2(y[rr][n][0])), pk2(lo2(y[rr][n][1])), pk2(hi2(y[rr][n][1]))};
    *(u32x4*)(lds + hwr) = hp[0][0];
    *(u32x4*)(lds + hwr + 16) = hp[1][0];
    if (m < 4) { *(u32x4*)(lds + hwr + 16 * 528) = hp[0][1]; *(u32x4*)(lds + hwr + 16 * 528 + 16) = hp[1][1]; }
    bar();
    STAMP(12);
    const u32 hb = OFF_H + (s2 >> 1) * 20480 + (s2 & 1) * 128;
#pragma unroll
    for (int n = 0; n < 3; ++n)
      if (n < 2 || w < 2) st1(lds4(lds + hrd + (w + 4 * n) * 1056), rs, hgo, hb + (w + 4 * n) * 2048);
    SB();
    float sm[2] = {0.f, 0.f}, sq[2] = {0.f, 0.f};
#pragma unroll
    for (int j = 0; j < 8; ++j)
#pragma unroll
      for (int rr = 0; rr < 2; ++rr) { const u32 hv = hp[rr][j >> 2][j & 3]; sm[rr] = dot2(hv, 0x3f803f80u, sm[rr]); sq[rr] = dot2(hv, hv, sq[rr]); }
    const float red = row_sum(fold16(fold32(sm[0], sq[0]), fold32(sm[1], sq[1]))) * (1.f / D);
    auto sw = __builtin_amdgcn_permlane32_swap(__float_as_uint(red), __float_as_uint(red), false, false);
    const float mean = __uint_as_float(sw[0]), var = fmaxf(__uint_as_float(sw[1]) - mean * mean, 0.f);
    const float mb1 = rbf(mean), sd1 = ln_std(var), rs1 = __builtin_amdgcn_rcpf(sd1);
    const float mb2 = rbf((mean - mb1) * rs1), sd2 = ln_std(var * rs1 * rs1);
    f32x4 o4 = {sd1 * sd2, -(mb1 + mb2 * sd1), mb1, rs1};
    tie(o4);
    arrive(lds, fl + OFF_FM, 3, 4, e + 3, l);
    STAMP(13);
    if (l == 0 || l == 16) st1(__builtin_bit_cast(u32x4, o4), rs, l, OFF_S1 + s2 * 128 + w * 32);
#pragma unroll
    for (int i = 0; i < 25; ++i) wr_[i] = lds4(lds + l16 + i * 1024);
    arrive(lds, fl + OFF_FS, 4, 4, e + 1, l);
  }
  f32x4 res[2][5];
  u32 hk[10], tpo, tso, av0, av1, so3[20], so4[27];
  {
    const int l = opq(l16) >> 4, m = l & 15, q = l >> 4;
    tpo = T * 20480 + kbp * 1024 + l16;
    tso = T * 20480 + (g * 5 + 2) * 1024 + (2 * (cb & 1) + (q >> 1)) * 256 + m * 16;
    const int rho = T * 16 + m, shift = (g & 1) ? -(1 + (g >> 1)) : (1 + (g >> 1));
    const int sp = (rho & 7) * 256 + (rho >> 3) - shift;
    const int rq = (sp & 255) * 8 + (sp >> 8);
    u32 av = (sp >= 0 && sp < S) ? (rq >> 4) * 20480 + q * 256 + (rq & 15) * 16 : 0x80000000u;
    asm volatile("" : "+v"(av), "+v"(tpo), "+v"(tso));
    if (w == 0) poll(rp + OFF_FM, pfirst, pn, e + 3, l);
    bar();
    STAMP(14);
    const int r0 = rot5(cb);
    f32x4 acc[5] = {z4, z4, z4, z4, z4};
    gemm12<0>(acc, wr_, lds + opq(l16), [&](int k) { int kk = k + r0; if (kk >= 20) kk -= 20; return ld1(rs, av, OFF_H + kk * 1024); });
    bar();
    STAMP(15);
    f32x4 cs[5];
#pragma unroll
    for (int i = 0; i < 5; ++i) cs[i] = ldsf(lds + L_MISC + q * 16 + i * 64);
    const f32x4 sc = ldsf(lds + L_ST + (w * 16 + m) * 16);
    const Gelu gc = gelu_consts(sc[0], sc[1]);
#pragma unroll
    for (int pp = 0; pp < 2; ++pp) st1(pkc(gelu4(acc[2 * pp], cs[2 * pp], gc), gelu4(acc[2 * pp + 1], cs[2 * pp + 1], gc)), rs, tpo, OFF_G1 + pp * 1024);
    const f32x4 g4 = gelu4(acc[4], cs[4], gc);
    const u32x4 so = solo_merge(pk2(lo2(g4)), pk2(hi2(g4)));
    if (!(q & 1)) st1(so, rs, tso, OFF_G1);
    STAMP(16);
    const u32x4 h0 = ld1(rs, tpo, OFF_H), h1 = ld1(rs, tpo, OFF_H + 1024);
    const u32x2 h2 = ld1h(rs, tso + (q & 1) * 8, OFF_H);
    const char* wrp = lds + opq(L_W2 - 1024 + 76800 + l16);
#pragma unroll
    for (int i = 0; i < 25; ++i) wr_[i] = lds4(wrp + i * 1024);
    arrive(lds, fl + OFF_FM, 5, 4, e + 4, l);
    STAMP(17);
    f32x2 xb[10];
#pragma unroll
    for (int i = 0; i < 10; ++i) {
      hk[i] = i < 4 ? h0[i] : i < 8 ? h1[i - 4] : h2[i - 8];
      xb[i] = up2(pk2(up2(pk2(up2(hk[i]) - sp2(sc[2]))) * sp2(sc[3])));
    }
#pragma unroll
    for (int r = 0; r < 4; ++r) { res[0][0][r] = xb[r][0]; res[0][1][r] = xb[r][1]; res[0][2][r] = xb[4 + r][0]; res[0][3][r] = xb[4 + r][1]; }
    res[0][4] = cat4(xb[8], xb[9]);
#pragma unroll
    for (int i = 0; i < 5; ++i) tie(res[0][i]);
    if (w == 0) poll(rp + OFF_FM, 8 * tb, 8, e + 4, l);
    bar();
    STAMP(18);
    gemm12<15>(res[0], wr_, lds + opq(L_W2 - 1024 + l16), [&](int k) { int kk = k + r0; if (kk >= 20) kk -= 20; return ld1(rs, l16, OFF_G1 + T * 20480 + kk * 1024); });
    bar();
    STAMP(19);
  }
  {
    const int l = opq(l16) >> 4, m = l & 15, q = l >> 4;
    const u32x4 x0 = pkc(res[0][0], res[0][1]), x1 = pkc(res[0][2], res[0][3]);
    const u32 x2 = pk2(lo2(res[0][4])), x3 = pk2(hi2(res[0][4]));
    st1(x0, rs, tpo, OFF_X1);
    st1(x1, rs, tpo, OFF_X1 + 1024);
    const u32x4 so = solo_merge(x2, x3);
    if (!(q & 1)) st1(so, rs, tso, OFF_X1);
    const u32 xk[10] = {x0[0], x0[1], x0[2], x0[3], x1[0], x1[1], x1[2], x1[3], x2, x3};
    float sm[2] = {0.f, 0.f}, sq[2] = {0.f, 0.f};
#pragma unroll
    for (int i = 0; i < 10; ++i) { sm[i & 1] = dot2(xk[i], 0x3f803f80u, sm[i & 1]); sq[i & 1] = dot2(xk[i], xk[i], sq[i & 1]); }
    float rd = fold32(sm[0] + sm[1], sq[0] + sq[1]);
    rd = fold16(rd, rd);
    if (!(q & 1)) __builtin_amdgcn_raw_buffer_store_b32(__float_as_uint(rd), rs, (q >> 1) * 64 + m * 4, OFF_S2 + T * 1024 + cb * 128, 16);
    STAMP(20);
    arrive(lds, fl + OFF_FM, 6, 4, e + 5, l);
    STAMP(21);
    f32x2 xb[10];
#pragma unroll
    for (int i = 0; i < 10; ++i) xb[i] = up2(xk[i]) + up2(hk[i]);
#pragma unroll
    for (int r = 0; r < 4; ++r) { res[0][0][r] = xb[r][0]; res[0][1][r] = xb[r][1]; res[0][2][r] = xb[4 + r][0]; res[0][3][r] = xb[4 + r][1]; }
    res[0][4] = cat4(xb[8], xb[9]);
#pragma unroll
    for (int i = 0; i < 5; ++i) tie(res[0][i]);
    av0 = OFF_X1 + (tb3 * 8 + 2 * w) * 20480 + l16; av1 = av0 + 20480;
    asm volatile("" : "+v"(av0), "+v"(av1));
#pragma unroll
    for (int k = 0; k < 20; ++k) { int kk = k + rot3(cb3); if (kk >= 20) kk -= 20; so3[k] = kk * 1024; asm volatile("" : "+s"(so3[k])); }
    if (w == 0) poll(rp + OFF_FM, 16 * tb3, 16, e + 5, l);
    bar();
    STAMP(22);
  }
  {
    const int T3 = tb3 * 8 + 2 * w;
    const int l = opq(l16) >> 4, m = l & 15, q = l >> 4;
    float st[2][4];
    f32x4 acc[2][7] = {{z4, z4, z4, z4, z4, z4, z4}, {z4, z4, z4, z4, z4, z4, z4}};
    Gelu gc[2];
    gemm32<20, 7, 6>(acc, lds, l16, [](int k) { return k * 7168; },
                     [&](auto KI, int j) { return ld1(rs, j ? av1 : av0, so3[KI.value]); },
                     [&](auto KI) {
      constexpr int k = KI.value;
      if constexpr (k == 4 || k == 9 || k == 14) bar_raw();
      if constexpr (k == 4) { STAMP(1); CSTAMP(0); }
      if constexpr (k == 19) { STAMP(2); CSTAMP(1); }
      if constexpr (k == 14) {
#pragma unroll
        for (int j = 0; j < 2; ++j)
#pragma unroll
          for (int i = 0; i < 4; ++i)
            st[j][i] = __uint_as_float(__builtin_amdgcn_raw_buffer_load_b32(rs, (q & 1) * 512 + (q >> 1) * 64 + m * 4, OFF_S2 + (T3 + j) * 1024 + i * 128, 16));
      }
    });
    f32x4 cs[7];
#pragma unroll
    for (int i = 0; i < 7; ++i) cs[i] = ldsf(lds + L_MISC + 320 + q * 16 + i * 64);
    bar();
    STAMP(23);
#pragma unroll
    for (int j = 0; j < 2; ++j) {
      float rd = (st[j][0] + st[j][1]) + (st[j][2] + st[j][3]);
      rd = fold16(rd, rd) * (1.f / D);
      auto sw = __builtin_amdgcn_permlane32_swap(__float_as_uint(rd), __float_as_uint(rd), false, false);
      const float mean = __uint_as_float(sw[0]);
      gc[j] = gelu_consts(ln_std(fmaxf(__uint_as_float(sw[1]) - mean * mean, 0.f)), -rbf(mean));
    }
    const int kb0 = (cb3 >> 1) * 7;
#pragma unroll
    for (int j = 0; j < 2; ++j) {
      const u32 gb = OFF_G3 + (T3 + j) * 55296 + kb0 * 1024;
#pragma unroll
      for (int pp = 0; pp < 3; ++pp)
        if (pp == 0 || cb3 < 15)
          st1(pkc(gelu4(acc[j][2 * pp], cs[2 * pp], gc[j]), gelu4(acc[j][2 * pp + 1], cs[2 * pp + 1], gc[j])), rs, l16, gb + ((cb3 & 1) * 4 + pp) * 1024);
      const f32x4 g6 = gelu4(acc[j][6], cs[6], gc[j]);
      const u32x4 so = solo_merge(pk2(lo2(g6)), pk2(hi2(g6)));
      if (!(q & 1)) st1(so, rs, (2 * (cb3 & 1) + (q >> 1)) * 256 + m * 16, gb + 3072);
    }
    STAMP(24);
    arrive(lds, fl + OFF_FM, 7, 4, e + 6, l);
    STAMP(25);
    av0 = OFF_G3 + T * 55296 + l16; av1 = OFF_G3 + (T ^ 1) * 55296 + l16;
    asm volatile("" : "+v"(av0), "+v"(av1));
#pragma unroll
    for (int k = 0; k < 27; ++k) { int kk = 2 * k + rot4(cb) + (w & 1); if (kk >= 54) kk -= 54; so4[k] = kk * 1024; asm volatile("" : "+s"(so4[k])); }
    if (w == 0) poll(rp + OFF_FM, 16 * tb3, 16, e + 6, l);
    bar();
    STAMP(26);
  }
  {
#pragma unroll
    for (int i = 0; i < 5; ++i) res[1][i] = z4;
    gemm32<27, 5, 6>(res, lds, (w & 1) * 5120 + l16, [](int k) { return (k % 15) * 10240; },
                     [&](auto KI, int j) { return ld1(rs, j ? av1 : av0, so4[KI.value]); },
                     [&](auto KI) {
      constexpr int k = KI.value;
      if constexpr (k == 3 || k == 7 || k == 11 || k == 15) bar_raw();
      if constexpr (k == 13) { STAMP(27); CSTAMP(2); }
    });
    STAMP(28); CSTAMP(3);
#pragma unroll
    for (int i = 0; i < 5; ++i) *(f32x4*)(lds + 122880 + w * 5120 + i * 1024 + l16) = res[1][i];
    bar();
#pragma unroll
    for (int i = 0; i < 5; ++i) res[0][i] += ldsf(lds + 122880 + (w ^ 1) * 5120 + i * 1024 + l16);
    const int l = opq(l16) >> 4;
    const int src = ((l & 3) * 16 + (l >> 2)) * 4;
    const int rho2 = T * 16 + (l >> 2);
    u16* op = out + ((rho2 & 7) * 256 + (rho2 >> 3)) * D;
#pragma unroll
    for (int pp = 0; pp < 2; ++pp) {
      u32x4 o = pkc(res[0][2 * pp], res[0][2 * pp + 1]);
#pragma unroll
      for (int r = 0; r < 4; ++r) o[r] = __builtin_amdgcn_ds_bpermute(src, o[r]);
      *(u32x4*)(op + (kbp + pp) * 32 + (l & 3) * 8) = o;
    }
    u32x2 so = {(u32)__builtin_amdgcn_ds_bpermute(src, pk2(lo2(res[0][4]))), (u32)__builtin_amdgcn_ds_bpermute(src, pk2(hi2(res[0][4])))};
    *(u32x2*)(op + (g * 5 + 2) * 32 + (cb & 1) * 16 + (l & 3) * 4) = so;
  }
  STAMP(29); STAMPV(30);
  PROF_END(0);
}
}

extern "C" void kernel_launch(void* const* d_in, const int* in_sizes, int n_in, void* d_out, int out_size, void* d_ws, size_t ws_size,
                              hipStream_t stream) {
  (void)hipFuncSetAttribute((const void*)fused_block, hipFuncAttributeMaxDynamicSharedMemorySize, L_TOT);
  fused_block<<<256, 512, L_TOT, stream>>>((const u16*)d_in[0], (const u16*)d_in[1], (const u16*)d_in[2], (const u16*)d_in[4], (const u16*)d_in[5],
                                           (const u16*)d_in[6], (const u16*)d_in[7], (char*)d_ws, (u16*)d_out);
}
```

```cpp
#include <hip/hip_runtime.h>
#include <stdint.h>
#include <type_traits>

typedef unsigned short u16;
typedef unsigned int u32;
typedef u32 u32x2 __attribute__((ext_vector_type(2)));
typedef u32 u32x4 __attribute__((ext_vector_type(4)));
typedef float f32x2 __attribute__((ext_vector_type(2)));
typedef float f32x4 __attribute__((ext_vector_type(4)));
typedef float f32x16 __attribute__((ext_vector_type(16)));
typedef __bf16 bf16x2 __attribute__((ext_vector_type(2)));
typedef __bf16 bf16x8 __attribute__((ext_vector_type(8)));
typedef __amdgpu_buffer_rsrc_t rsrc_t;
#define DI __device__ __forceinline__
#define LAS __attribute__((address_space(3)))
typedef LAS char lchar;
#define SB() __builtin_amdgcn_sched_barrier(0)

namespace {
typedef unsigned long long u64;
constexpr int S = 2048, D = 640, F = 1728;
constexpr u32 ACT = S * D * 2;
constexpr u32 FSTR = 256, FARR = 8 * 256 * FSTR;
constexpr u32 OFF_EP = 0;
constexpr u32 OFF_FM = 4096, OFF_FS = OFF_FM + FARR;
constexpr u32 OFF_S1 = OFF_FS + FARR;
constexpr u32 OFF_S2 = OFF_S1 + S * 16;
constexpr u32 OFF_X = OFF_S2 + 128 * 1024;
constexpr u32 OFF_T = OFF_X;
constexpr u32 OFF_H = OFF_T + 256 * 16384;
constexpr u32 OFF_G1 = OFF_H + ACT;
constexpr u32 OFF_X1 = OFF_G1 + ACT;
constexpr u32 OFF_G3 = OFF_X1 + ACT;
constexpr u32 OFF_W4 = OFF_G3 + S * F * 2;
constexpr u32 WS_USED = OFF_W4 + 8 * 276480;

struct Tab { u32 v[256]; };
constexpr double c_sin0(double x) { double t = x, s = x; for (int i = 1; i < 10; ++i) { t *= -x * x / ((2 * i) * (2 * i + 1)); s += t; } return s; }
constexpr double c_cos0(double x) { double t = 1, s = 1; for (int i = 1; i < 10; ++i) { t *= -x * x / ((2 * i - 1) * (2 * i)); s += t; } return s; }
constexpr double c_cos(int n, int den) {
  n = (4 * n) % (4 * den);
  const int qd = n / den, r = n % den;
  const double x = 1.5707963267948966 * r / den;
  return qd == 0 ? c_cos0(x) : qd == 1 ? -c_sin0(x) : qd == 2 ? -c_cos0(x) : c_sin0(x);
}
constexpr double c_sin(int n, int den) {
  n = (4 * n) % (4 * den);
  const int qd = n / den, r = n % den;
  const double x = 1.5707963267948966 * r / den;
  return qd == 0 ? c_sin0(x) : qd == 1 ? c_cos0(x) : qd == 2 ? -c_sin0(x) : -c_cos0(x);
}
constexpr u32 c_f32(double x) { return __builtin_bit_cast(u32, (float)x); }
constexpr u32 c_bf(double x) { u32 u = c_f32(x); u += 0x7fffu + ((u >> 16) & 1u); return u >> 16; }
constexpr u32 c_pk(double lo, double hi) { return c_bf(lo) | (c_bf(hi) << 16); }
constexpr Tab make_tab(int id) {
  Tab T{};
  for (int l = 0; l < 64; ++l)
    for (int j = 0; j < 4; ++j) {
      const int m = l & 15, q = l >> 4;
      u32 v = 0;
      if (id < 2) {
        double e[2] = {0, 0};
        for (int h = 0; h < 2; ++h) { int jj = 2 * j + h; if (jj < 5) e[h] = id ? -c_sin((5 * q + jj) * m, 20) : c_cos((5 * q + jj) * m, 20); }
        v = c_pk(e[0], e[1]);
      } else if (id < 8) {
        const int k2 = 16 * (id & 1) + m, n = 8 * q + 2 * j;
        v = id < 4 ? c_pk(c_cos(k2 * n, 32), c_cos(k2 * (n + 1), 32))
                   : id < 6 ? c_pk(c_sin(k2 * n, 32), c_sin(k2 * (n + 1), 32)) : c_pk(-c_sin(k2 * n, 32), -c_sin(k2 * (n + 1), 32));
      } else if (id < 12) {
        const int a = (8 * q + 2 * j + (id & 1)) * m;
        v = c_f32(id < 10 ? c_cos(a, 640) : c_sin(a, 640));
      } else if (id < 16) {
        const int a = (4 * q + j) * m;
        v = id == 12 ? c_pk(c_cos(a, 16), c_sin(a, 16)) : id == 13 ? c_pk(-c_sin(a, 16), c_cos(a, 16)) : c_f32(id == 14 ? c_cos(a, 256) : c_sin(a, 256));
      } else if (id < 24) {
        const int t = (id >> 1) & 1, kap = id & 1, n2 = 16 * t + m, a = (16 * kap + 4 * q + j) * n2;
        v = id < 20 ? c_pk(c_cos(a, 32), c_sin(a, 32)) : c_pk(-c_sin(a, 32), c_cos(a, 32));
      } else if (id < 26) {
        const int k1 = 4 * q + j, n1 = 16 * (id & 1) + m;
        const double wg = (k1 == 0 || k1 == 10) ? 1. : (k1 < 10 ? 2. : 0.), sw = (k1 > 0 && k1 < 10) ? 2. : 0.;
        v = n1 < 20 ? c_pk(wg * c_cos(k1 * n1, 20), sw * c_sin(k1 * n1, 20)) : 0u;
      } else if (id < 30) {
        const int n2 = 16 * (id & 1) + m, a = (4 * q + j) * n2;
        v = c_f32((id < 28 ? c_cos(a, 640) : c_sin(a, 640)) / (2048. * 640.));
      } else {
        const int n2 = 16 * (id & 1) + m;
        v = (8 * q + 2 * j == n2 ? 0x3f80u : 0u) | (8 * q + 2 * j + 1 == n2 ? 0x3f800000u : 0u);
      }
      T.v[l * 4 + j] = v;
    }
  return T;
}
template <int I> constexpr Tab tab_c = make_tab(I);
__device__ const __attribute__((aligned(1024))) Tab g_tab[32] = {
    tab_c<0>, tab_c<1>, tab_c<2>, tab_c<3>, tab_c<4>, tab_c<5>, tab_c<6>, tab_c<7>, tab_c<8>, tab_c<9>, tab_c<10>,
    tab_c<11>, tab_c<12>, tab_c<13>, tab_c<14>, tab_c<15>, tab_c<16>, tab_c<17>, tab_c<18>, tab_c<19>, tab_c<20>, tab_c<21>,
    tab_c<22>, tab_c<23>, tab_c<24>, tab_c<25>, tab_c<26>, tab_c<27>, tab_c<28>, tab_c<29>, tab_c<30>, tab_c<31>};

template <int I, int N, class Fn> DI void sfor(Fn&& f) {
  if constexpr (I < N) { f(std::integral_constant<int, I>{}); sfor<I + 1, N>(f); }
}
DI float bflo(u32 v) { return __uint_as_float(v << 16); }
DI float bfhi(u32 v) { return __uint_as_float(v & 0xffff0000u); }
DI f32x2 up2(u32 v) { return f32x2{bflo(v), bfhi(v)}; }
DI u32 pk2(f32x2 v) { return __builtin_bit_cast(u32, __builtin_convertvector(v, bf16x2)); }
DI u32 pk(float lo, float hi) { return pk2(f32x2{lo, hi}); }
DI float rbf(float x) { return bflo(pk(x, x)); }
DI f32x2 fma2(f32x2 a, f32x2 b, f32x2 c) { return __builtin_elementwise_fma(a, b, c); }
DI f32x2 sp2(float a) { return f32x2{a, a}; }
DI f32x2 lo2(f32x4 a) { return f32x2{a[0], a[1]}; }
DI f32x2 hi2(f32x4 a) { return f32x2{a[2], a[3]}; }
DI f32x4 cat4(f32x2 a, f32x2 b) { return f32x4{a[0], a[1], b[0], b[1]}; }
DI float dot2(u32 a, u32 b, float c) { return __builtin_amdgcn_fdot2_f32_bf16(__builtin_bit_cast(bf16x2, a), __builtin_bit_cast(bf16x2, b), c, false); }
DI f32x4 mfma16(u32x4 a, u32x4 b, f32x4 c) {
  return __builtin_amdgcn_mfma_f32_16x16x32_bf16(__builtin_bit_cast(bf16x8, a), __builtin_bit_cast(bf16x8, b), c, 0, 0, 0);
}
DI f32x16 mfma32(u32x4 a, u32x4 b, f32x16 c) {
  return __builtin_amdgcn_mfma_f32_32x32x16_bf16(__builtin_bit_cast(bf16x8, a), __builtin_bit_cast(bf16x8, b), c, 0, 0, 0);
}
template <int CTRL> DI float dpp(float x) {
  return __builtin_bit_cast(float, __builtin_amdgcn_mov_dpp(__builtin_bit_cast(int, x), CTRL, 0xf, 0xf, true));
}
DI float row_sum(float x) {
  x += dpp<0xB1>(x);
  x += dpp<0x4E>(x);
  x += dpp<0x141>(x);
  x += dpp<0x140>(x);
  return x;
}
DI float fold32(float a, float b) {
  auto t = __builtin_amdgcn_permlane32_swap(__float_as_uint(a), __float_as_uint(b), false, false);
  return __uint_as_float(t[0]) + __uint_as_float(t[1]);
}
DI float fold16(float a, float b) {
  auto t = __builtin_amdgcn_permlane16_swap(__float_as_uint(a), __float_as_uint(b), false, false);
  return __uint_as_float(t[0]) + __uint_as_float(t[1]);
}
DI float rdl(float x, int lane) { return __uint_as_float(__builtin_amdgcn_readlane(__float_as_uint(x), lane)); }
DI float ln_std(float var) { return rbf(__builtin_amdgcn_sqrtf(rbf(rbf(var) + 1e-5f))); }
DI void rot4(f32x4& re, f32x4& im, f32x4 a, f32x4 b, f32x4 c, f32x4 d) {
  f32x2 r0 = fma2(lo2(re), lo2(a), lo2(im) * lo2(b)), r1 = fma2(hi2(re), hi2(a), hi2(im) * hi2(b));
  f32x2 i0 = fma2(lo2(re), lo2(c), lo2(im) * lo2(d)), i1 = fma2(hi2(re), hi2(c), hi2(im) * hi2(d));
  re = cat4(r0, r1); im = cat4(i0, i1);
}
DI f32x4 asf(u32x4 a) { return __builtin_bit_cast(f32x4, a); }
DI f32x4 neg4(u32x4 a) { f32x4 r = asf(a ^ 0x80000000u); asm volatile("" : "+v"(r)); return r; }
DI u32x4 pkc(f32x4 a, f32x4 b) { return u32x4{pk(a[0], b[0]), pk(a[1], b[1]), pk(a[2], b[2]), pk(a[3], b[3])}; }
typedef f32x4 Gelu;
DI Gelu gelu_consts(float s, float ga) {
  const float c0 = -2.f * 0.7978845608028654f * 1.4426950408889634f, c1 = c0 * 0.044715f, rs = __builtin_amdgcn_rcpf(s);
  return Gelu{c0 * rs, c1 * rs * rs * rs, s, ga};
}
DI f32x2 gelu2(f32x2 a, Gelu g) {
  f32x2 z = fma2(a * a, sp2(g[1]), sp2(g[0])) * a;
  f32x2 e = {__builtin_amdgcn_exp2f(z[0]), __builtin_amdgcn_exp2f(z[1])};
  f32x2 d = fma2(e, sp2(g[2]), sp2(g[2]));
  return a * f32x2{__builtin_amdgcn_rcpf(d[0]), __builtin_amdgcn_rcpf(d[1])};
}
DI f32x4 gelu4(f32x4 a, f32x4 cs, Gelu g) {
  return cat4(gelu2(fma2(lo2(cs), sp2(g[3]), lo2(a)), g), gelu2(fma2(hi2(cs), sp2(g[3]), hi2(a)), g));
}
DI u32 opq(u32 v) { asm volatile("" : "+v"(v)); return v; }
DI void tie(u32x4& a) { asm volatile("" : "+v"(a)); }
DI void tie(f32x4& a) { asm volatile("" : "+v"(a)); }
template <int N> DI void wait_vm() { asm volatile("s_waitcnt vmcnt(%0)" ::"i"(N) : "memory"); }
DI void bar() { asm volatile("s_waitcnt lgkmcnt(0)\n\ts_barrier" ::: "memory"); }
DI u32x4 ld1(rsrc_t r, u32 voff, u32 soff) { return __builtin_amdgcn_raw_buffer_load_b128(r, voff, soff, 16); }
DI u32x2 ld1h(rsrc_t r, u32 voff, u32 soff) { return __builtin_amdgcn_raw_buffer_load_b64(r, voff, soff, 16); }
DI void st1(u32x4 v, rsrc_t r, u32 voff, u32 soff) {
  __builtin_amdgcn_raw_buffer_store_b128(v, r, voff, soff, 16);
  asm volatile("s_nop 1" ::"v"(v) : "memory");
}
DI u32x4 lds4(const lchar* p) { return *(const LAS u32x4*)p; }
DI f32x4 ldsf(const lchar* p) { return *(const LAS f32x4*)p; }
DI void glds(u32 ldsaddr, const void* sbase, u32 voff) {
  asm volatile("s_mov_b32 m0, %0\n\ts_nop 4\n\tglobal_load_lds_dwordx4 %1, %2" ::"s"(ldsaddr), "v"(voff), "s"(sbase) : "memory");
}
DI void gldsx(u32 ldsaddr, const void* sbase, u32 voff) {
  asm volatile("s_mov_b32 m0, %0\n\ts_nop 4\n\tglobal_load_lds_dwordx4 %1, %2 nt" ::"s"(ldsaddr), "v"(voff), "s"(sbase) : "memory");
}
DI void glds5s(u32 ldsaddr, const void* sbase, u32 voff) {
#define GL "\n\tglobal_load_lds_dwordx4 %1, %2"
  asm volatile("s_mov_b32 m0, %0\n\ts_nop 4" GL " sc1" GL " offset:1024 sc1" GL " offset:2048 sc1" GL " offset:3072 sc1" ::"s"(ldsaddr), "v"(voff), "s"(sbase) : "memory");
  asm volatile("s_mov_b32 m0, %0\n\ts_nop 4" GL " sc1" ::"s"(ldsaddr + 4096), "v"(voff), "s"((const char*)sbase + 4096) : "memory");
}
DI void touch(u32 dummy, const char* p, u32 voff) {
  asm volatile("s_mov_b32 m0, %0\n\ts_nop 4\n\tglobal_load_lds_dword %1, %2" ::"s"(dummy), "v"(voff), "s"(p) : "memory");
}
typedef short s16x4 __attribute__((ext_vector_type(4)));
DI u32x2 trd(const lchar* p) {
  return __builtin_bit_cast(u32x2, __builtin_amdgcn_ds_read_tr16_b64_v4i16((LAS s16x4*)p));
}
DI u32x4 pk44(f32x4 a, f32x4 b) { return u32x4{pk2(lo2(a)), pk2(hi2(a)), pk2(lo2(b)), pk2(hi2(b))}; }
DI void st1h(u32x2 v, rsrc_t r, u32 voff, u32 soff) { __builtin_amdgcn_raw_buffer_store_b64(v, r, voff, soff, 16); }
DI int g3pos(int kb) { return kb < 27 ? kb : kb == 27 ? 53 : kb - 1; }
DI int wrapk(int k, int n) { return k >= n ? k - n : k; }

template <bool FWD> DI u32x4 radix8_const(int l, int s2) {
  const int i = l & 31, h = l >> 5;
  const int o = (i & 4) + ((i >> 1) & 1) + ((i >> 2) & 2), c = i & 1;
  const float fo = (float)o;
  const float a0 = FWD ? fo * (float)(1024 * h + s2) * (1.f / 2048.f) : (float)(4 * h) * (fo * 256.f + (float)s2) * (1.f / 2048.f);
  const float da = FWD ? fo * 0.125f : (fo * 256.f + (float)s2) * (1.f / 2048.f);
  u32x4 r;
#pragma unroll
  for (int jj = 0; jj < 4; ++jj) {
    float a = a0 + (c ? 0.25f : 0.f) + jj * da;
    u32 v = pk(__builtin_amdgcn_cosf(a), __builtin_amdgcn_sinf(a));
    r[jj] = (i < 16) ? v : 0u;
  }
  return r;
}

constexpr int K1_TS = 1040, K3_KS = 144, K3_RS = 12 * 144;
constexpr int L_A = 102400;
constexpr int L_XR = 113856;
constexpr int L_B = 124096;
constexpr int L_WF = 140480;
constexpr int L_MISC = 159744, L_TOT = 163840;
constexpr int L_CNT = L_MISC + 768, L_ZERO = L_MISC + 1008, L_ST = L_MISC + 1024, L_CSP = L_MISC + 2048, L_DUMMY = L_MISC + 3840;
static_assert(L_A + 11 * K1_TS <= L_XR && L_XR + 10240 <= L_B && L_B + 16384 <= L_WF && L_WF + 16384 <= L_MISC, "lds map");
constexpr int L_T3 = 81920;
constexpr int SPIN_MAX = 1 << 16;
constexpr int CODE_KB = 48;

enum { C_TB, C_P1, C_ST, C_WF, C_P2A, C_P2B, C_B1, C_B2, C_F3, C_FS, C_F4, C_F6, C_FW, C_S1, C_CS, C_R1, C_R2, C_R3, C_R4, C_E1,
       C_W1, C_W2, C_W3, C_W4, C_EP1, C_EP3, C_G3, C_NUM = C_G3 + 4 };
static_assert(C_NUM <= 60, "counters");
template <int C> DI void sig_(u32 cb) {
  asm volatile("s_mov_b64 exec, 1\n\tds_inc_u32 %0, %0 offset:%1\n\ts_mov_b64 exec, -1" ::"v"(cb), "i"(C * 4) : "memory");
}
template <int C, int N, int SLEEP> DI void await_(u32 cb) {
  u32 v, it;
  if constexpr (SLEEP)
    asm volatile("s_mov_b32 %1, 0x400000\n1:\n\tds_read_b32 %0, %2 offset:%3\n\ts_sub_u32 %1, %1, 1\n\ts_cbranch_scc1 2f\n\ts_waitcnt lgkmcnt(0)\n\t"
                 "v_cmp_gt_u32 vcc, %4, %0\n\ts_cbranch_vccz 2f\n\ts_sleep 1\n\ts_branch 1b\n2:\n\ts_waitcnt lgkmcnt(0)"
                 : "=&v"(v), "=&s"(it) : "v"(cb), "i"(C * 4), "i"(N) : "memory", "vcc", "scc");
  else
    asm volatile("s_mov_b32 %1, 0x400000\n1:\n\tds_read_b32 %0, %2 offset:%3\n\ts_sub_u32 %1, %1, 1\n\ts_cbranch_scc1 2f\n\ts_waitcnt lgkmcnt(0)\n\t"
                 "v_cmp_gt_u32 vcc, %4, %0\n\ts_cbranch_vccnz 1b\n2:\n\ts_waitcnt lgkmcnt(0)"
                 : "=&v"(v), "=&s"(it) : "v"(cb), "i"(C * 4), "i"(N) : "memory", "vcc", "scc");
}
#define SIG(c) sig_<c>(cb_)
#define AWAIT(c, n) await_<c, n, 0>(cb_)
#define HWAIT(c, n) await_<c, n, 1>(cb_)
#define CBAR(c) do { SIG(c); AWAIT(c, 4); } while (0)
DI u32 peek(const lchar* lds, int c) { return __hip_atomic_load((const LAS u32*)(lds + L_CNT + c * 4), __ATOMIC_RELAXED, __HIP_MEMORY_SCOPE_WORKGROUP); }
#define CHECK(v, c, n) do { if (__builtin_amdgcn_readfirstlane(v) < n) AWAIT(c, n); asm volatile("" ::: "memory"); } while (0)
template <int C> DI void arrive(u32 cb, char* flag, u32 nw, u64 val, int l) {
  u32 old;
  asm volatile("s_waitcnt vmcnt(0)\n\ts_mov_b64 exec, 1\n\tds_inc_rtn_u32 %0, %1, %1 offset:%2\n\ts_mov_b64 exec, -1\n\ts_waitcnt lgkmcnt(0)"
               : "=&v"(old) : "v"(cb), "i"(C * 4) : "memory");
  if (__builtin_amdgcn_readfirstlane(old) == nw - 1 && l < 8)
    __hip_atomic_store((u64*)(flag + l * (256 * FSTR)), val, __ATOMIC_RELAXED, __HIP_MEMORY_SCOPE_AGENT);
}
DI void load_tagged(u32x4 (&v)[4], rsrc_t rs, u32 voff, u32 soff, u32 step, u32 tag) {
#pragma unroll 1
  for (int s = 0; s < SPIN_MAX; ++s) {
#pragma unroll
    for (int i = 0; i < 4; ++i) v[i] = ld1(rs, voff, soff + i * step);
    int bad = ((v[0][3] ^ tag) | (v[1][3] ^ tag)) | ((v[2][3] ^ tag) | (v[3][3] ^ tag));
    bad |= __builtin_amdgcn_mov_dpp(bad, 0xB1, 0xf, 0xf, true);
    bad |= __builtin_amdgcn_mov_dpp(bad, 0x4E, 0xf, 0xf, true);
    if (bad == 0) break;
    asm volatile("" ::: "memory");
  }
}
DI void poll(const char* rep, int first, int n, u64 want, int l) {
  if (l < n) {
    const u64* p = (const u64*)(rep + ((first + l) & 255) * FSTR);
#pragma unroll 1
    for (int s = 0; s < SPIN_MAX; ++s)
      if (__hip_atomic_load(p, __ATOMIC_RELAXED, __HIP_MEMORY_SCOPE_AGENT) - want < 8ull) break;
  }
  asm volatile("" ::: "memory");
}
template <int N> DI void wait_lgkm() { __builtin_amdgcn_s_waitcnt(0xC07F | (N << 8)); }
template <int NK, int NT, int W, int NB, class OffFn, class LdFn, class HookFn>
DI void gemm32(f32x4 (&acc)[2][NT], const lchar* lds, u32 p0, OffFn off, LdFn load, HookFn hook) {
  constexpr int H = (NT + 1) / 2, HI = 512 * NT;
  u32x4 act[W][2];
  u32x2 wf[2][2 * NT];
  sfor<0, W>([&](auto KI) { act[KI.value][0] = load(KI.value, 0); SB(); act[KI.value][1] = load(KI.value, 1); SB(); });
  const lchar* pb[NB];
#pragma unroll
  for (int i = 0; i < NB; ++i) pb[i] = lds + opq(p0 + i * 61440);
#pragma unroll
  for (int n = 0; n < 2 * NT; ++n) { const int o = off(0) + (n >> 1) * 32 + (n & 1) * HI; wf[0][n] = trd(pb[o / 61440] + o % 61440); }
  SB();
  sfor<0, NK>([&](auto KI) {
    constexpr int k = KI.value;
    sfor<0, 2 * NT>([&](auto II) {
      constexpr int n = II.value, i = n >> 1, j = n & 1;
      if constexpr (n == 0) { wait_lgkm<2 * (NT - H)>(); SB(); }
      if constexpr (n == 2 * H) { wait_lgkm<(k + 1 < NK) ? 2 * H : 0>(); SB(); }
      acc[j][i] = mfma16(u32x4{wf[k & 1][2 * i][0], wf[k & 1][2 * i][1], wf[k & 1][2 * i + 1][0], wf[k & 1][2 * i + 1][1]}, act[k % W][j], acc[j][i]);
      SB();
      if constexpr (k + 1 < NK) { constexpr int o = off(k + 1) + i * 32 + j * HI; wf[(k + 1) & 1][n] = trd(pb[o / 61440] + o % 61440); }
      if constexpr ((n == 1 || n == 3) && k >= 1 && k - 1 + W < NK) act[(k - 1) % W][n >> 1] = load(k - 1 + W, n >> 1);
      SB();
    });
    hook(KI);
    SB();
  });
}
template <int NT> DI void colsum_step(f32x4 (&cs)[NT], const lchar* p) {
  const u32x4 ones = {0x3f803f80u, 0x3f803f80u, 0x3f803f80u, 0x3f803f80u};
  u32x2 f[2 * NT];
#pragma unroll
  for (int n = 0; n < 2 * NT; ++n) f[n] = trd(p + (n >> 1) * 32 + (n & 1) * 512 * NT);
#pragma unroll
  for (int i = 0; i < NT; ++i) cs[i] = mfma16(u32x4{f[2 * i][0], f[2 * i][1], f[2 * i + 1][0], f[2 * i + 1][1]}, ones, cs[i]);
}

__global__ __launch_bounds__(512, 2) void fused_block(const u16* __restrict__ x, const u16* __restrict__ wr, const u16* __restrict__ wi,
                                                      const u16* __restrict__ w1, const u16* __restrict__ w2, const u16* __restrict__ w3,
                                                      const u16* __restrict__ w4, char* __restrict__ ws, u16* __restrict__ out) {
  lchar* const lds = (lchar*)0;
  const char* pc0;
  asm volatile("s_getpc_b64 %0" : "=s"(pc0));
  const int b = blockIdx.x, xcd = b & 7, i5 = b >> 3;
  const int tid = threadIdx.x, w = __builtin_amdgcn_readfirstlane(tid >> 6), l = tid & 63;
  const int s2 = xcd * 32 + i5, cb = i5 & 7, tb3 = (xcd >> 1) * 4 + (i5 >> 3), tb = 2 * tb3 + (xcd & 1), cb3 = (xcd & 1) * 8 + cb;
  const int lidA = (xcd & 6) * 32 + (i5 & 24);
  const int hw = w & 3, g = cb >> 1;
  constexpr u32 lb = 0;
  const u32 l16 = l * 16;
  u32 cb_ = L_CNT;
  asm volatile("" : "+v"(cb_));
  if (w == 4 && l < CODE_KB / 4) touch(lb + L_DUMMY, pc0 + i5 * (CODE_KB * 32), l * 128);
  if (w < 4) {
#pragma unroll
    for (int n = 0; n < 8; ++n) glds(lb + (n < 4 ? L_B : L_T3 - 16384) + (4 * n + hw) * 1024, (const char*)g_tab + (4 * n + hw) * 1024, l16);
  }
  if (w == 6) *(LAS u32*)(lds + L_CNT + l * 4) = 0u;
  asm volatile("" ::"s"(x), "s"(wr), "s"(wi), "s"(w1), "s"(w2), "s"(w3), "s"(w4), "s"(ws), "s"(out));
  if (w == 7 && l < 16) touch(lb + L_DUMMY, ws, OFF_FM + s2 * FSTR + l * (256 * FSTR));
  if (w < 4) {
    gldsx(lb + L_XR + w * 2560, x + (2 * w * 256 + s2) * D, l16);
    gldsx(lb + L_XR + w * 2560 + 1024, x + (2 * w * 256 + s2) * D, l < 16 ? 1024 + l16 : 256 * D * 2 - 256 + l16);
    if (l < 32) gldsx(lb + L_XR + w * 2560 + 2048, x + ((2 * w + 1) * 256 + s2) * D, 768 + l16);
  }
  bar();
  const rsrc_t rs = __builtin_amdgcn_make_buffer_rsrc(ws, 0, WS_USED, 0x00020000);
  const u64 ep = __hip_atomic_load((const u64*)(ws + OFF_EP), __ATOMIC_RELAXED, __HIP_MEMORY_SCOPE_AGENT);
  char* fl = ws + s2 * FSTR;
  const char* rp = ws + xcd * (256 * FSTR);
  const bool tA = !(tb & 1);
  const int pfirst = 16 * tb3 - ((g & 1) ? 0 : 1 + (g >> 1)), pn = 17 + (g >> 1);
  const int r5 = 5 * ((cb + tb3) & 3), r3 = r5;
  if (w >= 4) {
    const f32x4 z4 = {0, 0, 0, 0};
    {
      const int u = xcd + 8 * ((i5 & 15) * 16 + (hw >> 1) * 8 + (l >> 3)), uu = i5 < 16 ? u : ((S - u) & (S - 1));
      const int o = (l & 7) < 5 ? (l & 7) * 128 : 636;
      touch(lb + L_DUMMY, (const char*)((hw & 1) ? wi : wr), (uu * 642 + o) & ~3);
    }
    auto load_filter = [&]() {
      const int boff = i5 < 16 ? 40 * i5 : 1252 - 40 * i5, mc = (l ^ (l >> 3)) & 3;
#pragma unroll
      for (int n = 0; n < 4; ++n) {
        const int u = xcd + 8 * ((hw * 4 + n) * 16 + (l >> 2)), uu = i5 < 16 ? u : ((S - u) & (S - 1));
        const char* src = (const char*)((mc & 2) ? wi : wr) + ((uu * 642 + boff) & ~3) + (mc & 1) * 16;
        asm volatile("s_mov_b32 m0, %0\n\ts_nop 4\n\tglobal_load_lds_dwordx4 %1, off" ::"s"(lb + L_WF + (hw * 4 + n) * 1024), "v"(src) : "memory");
      }
    };
    u32 v5[5], v7[7];
#pragma unroll
    for (int n = 0; n < 5; ++n) {
      const int c = n * 64 + l, rm = c / 10, cc = c - 10 * rm;
      v5[n] = rm * (D * 2) + wrapk(cc + 2 * (cb & 1), 10) * 16;
    }
    auto src5 = [&](const u16* W, int k, int r, int ks) { return (const char*)W + wrapk(k + r, ks) * (32 * D * 2) + cb * 160; };
    auto src3 = [&](int k) { return (const char*)w3 + wrapk(k + r3, 20) * (32 * F * 2) + cb3 * 224; };
    auto dma5 = [&](const u16* W, int k, int r, int ks, int dst) {
      const char* p = src5(W, k, r, ks);
#pragma unroll
      for (int n = 0; n < 5; ++n) glds(lb + dst + n * 1024, p, v5[n]);
    };
    auto ld5 = [&](u32x4* d, const u16* W, int k, int r, int ks) {
      const char* p = src5(W, k, r, ks);
#pragma unroll
      for (int n = 0; n < 5; ++n) d[n] = __builtin_nontemporal_load((const u32x4*)(p + v5[n]));
    };
    auto ld7 = [&](u32x4* d, int k) {
      const char* p = src3(k);
#pragma unroll
      for (int n = 0; n < 7; ++n) d[n] = *(const u32x4*)(p + v7[n]);
    };
    const u32 dm = lb + L_DUMMY;
    const u64 e = (ep + 1) << 8;
    const u16* w12 = tA ? w1 : w2;
    auto round5 = [&](int i) { dma5(w12, 4 * i + hw, r5, 20, (4 * i + hw) * 5120); };
    if (hw < 2) touch(dm, (const char*)(hw ? w2 : w1) + s2 * 3200, (l < 25 ? l : 0) * 128);
    HWAIT(C_ST, 4);
    load_filter();
    wait_vm<0>();
    SIG(C_WF);
#pragma unroll
    for (int n = 0; n < 7; ++n) {
      const int c = n * 64 + l, rm = c / 14, cc = wrapk(c - 14 * rm + 2 * (cb3 & 1), 14);
      v7[n] = rm * (F * 2) + (cb3 == 15 && cc >= 6 ? 0 : cc * 16);
    }
    const u32 t5 = ((l >> 4) * 4 + ((l >> 2) & 3)) * 160 + (l & 3) * 8, t7 = ((l >> 4) * 4 + ((l >> 2) & 3)) * 224 + (l & 3) * 8;
    auto cs_sum = [&](int c, int dst) {
      const LAS float* p = (const LAS float*)(lds + L_CSP) + c;
      *(LAS float*)(lds + L_MISC + dst + c * 4) = (p[0] + p[112]) + (p[224] + p[336]);
    };
    if (tA) {
      HWAIT(C_P2A, 4);
#pragma unroll 1
      for (int i = 0; i < 3; ++i) round5(i);
      HWAIT(C_B1, 4);
#pragma unroll 1
      for (int i = 3; i < 5; ++i) round5(i);
      wait_vm<0>();
      SIG(C_W1);
      f32x4 cs[5] = {z4, z4, z4, z4, z4};
#pragma unroll 1
      for (int i = 0; i < 5; ++i) colsum_step<5>(cs, lds + (4 * i + hw) * 5120 + t5);
      if ((l & 15) == 0) {
#pragma unroll
        for (int i = 0; i < 5; ++i) *(LAS f32x4*)(lds + L_CSP + hw * 448 + i * 64 + l) = cs[i];
      }
      CBAR(C_S1);
      if (hw * 64 + l < 80) cs_sum(hw * 64 + l, 0);
      HWAIT(C_FS, 4);
      if (hw == 0) {
        if (l < 8) st1(lds4(lds + L_XR + l16), rs, l16, OFF_S1 + s2 * 128);
        wait_vm<0>();
        if (l < 8) __hip_atomic_store((u64*)(fl + OFF_FS + l * (256 * FSTR)), e + 1, __ATOMIC_RELAXED, __HIP_MEMORY_SCOPE_AGENT);
      }
      if (hw >= 2) {
        HWAIT(C_R1, 1);
        poll(rp + OFF_FS, pfirst, pn, e + 1, l);
        const int rho = tb3 * 128 + (hw & 1) * 64 + l, shift = (g & 1) ? -(1 + (g >> 1)) : (1 + (g >> 1));
        const int sp = (rho & 7) * 256 + (rho >> 3) - shift;
        const bool ok = sp >= 0 && sp < S;
        const u32x2 ab = ld1h(rs, ok ? ((sp & 255) * 8 + (sp >> 8)) * 16 : 0x80000000u, OFF_S1);
        *(LAS u32x2*)(lds + L_ST + ((hw & 1) * 64 + l) * 8) = u32x2{ok ? ab[0] : 0x3f800000u, ab[1]};
      }
      SIG(C_EP1);
      HWAIT(C_F4, 4);
      const int rk = tb3 * 4 + hw;
      if (rk < 54) {
        u32x4 r4[5];
        ld5(r4, w4, rk, 0, 54);
#pragma unroll
        for (int n = 0; n < 5; ++n) st1(r4[n], rs, l16, OFF_W4 + (cb * 54 + g3pos(rk)) * 5120 + n * 1024);
      }
#pragma unroll 1
      for (int i = 0; i < 5; ++i) {
        const char* p = src3(4 * i + hw);
#pragma unroll
        for (int n = 0; n < 7; ++n) glds(lb + (4 * (i ? i - 1 : 4) + hw) * 7168 + n * 1024, p, v7[n]);
      }
      arrive<C_FW>(cb_, fl + OFF_FS + 8, 4, e + 1, l);
      SIG(C_W2);
    } else {
      u32x4 rb[28];
      HWAIT(C_B1, 4);
      touch(dm, (const char*)w3 + (tb3 * 8 + cb) * 17280 + hw * 4320, (l < 34 ? l : 0) * 128);
      HWAIT(C_F3, 4);
      __builtin_amdgcn_s_sleep(24);
#pragma unroll 1
      for (int i = 0; i < 5; ++i) round5(i);
      {
        const char* p = src3(hw);
#pragma unroll
        for (int n = 0; n < 7; ++n) glds(lb + (16 + hw) * 7168 + n * 1024, p, v7[n]);
      }
#pragma unroll
      for (int i = 0; i < 4; ++i) ld7(rb + 7 * i, 4 * i + 4 + hw);
      wait_vm<0>();
      SIG(C_W1);
      SIG(C_W2);
      HWAIT(C_E1, 4);
#pragma unroll
      for (int i = 0; i < 28; ++i) *(LAS u32x4*)(lds + hw * 7168 + l16 + (i / 7) * 28672 + (i % 7) * 1024) = rb[i];
    }
    SIG(C_W3);
    {
      f32x4 c3[7] = {z4, z4, z4, z4, z4, z4, z4};
#pragma unroll 1
      for (int i = 0; i < 5; ++i) colsum_step<7>(c3, lds + (4 * i + hw) * 7168 + t7);
      if ((l & 15) == 0) {
#pragma unroll
        for (int i = 0; i < 7; ++i) *(LAS f32x4*)(lds + L_CSP + hw * 448 + i * 64 + l) = c3[i];
      }
    }
    CBAR(C_CS);
    if (hw * 64 + l < 112) cs_sum(hw * 64 + l, 320);
    const char* w4p = ws + OFF_W4 + cb * 276480 + (tb & 1) * 138240;
    HWAIT(C_R3, 1);
    if (hw >= 2) {
      const u32 vo = OFF_S2 + (tb3 * 8 + (hw & 1) * 4 + (l >> 4)) * 1024 + (l & 15) * 4;
      float a[16];
#pragma unroll
      for (int i = 0; i < 16; ++i) a[i] = __uint_as_float(__builtin_amdgcn_raw_buffer_load_b32(rs, vo, i * 64, 16));
      float sm = 0.f, sq = 0.f;
#pragma unroll
      for (int i = 0; i < 8; ++i) { sm += a[2 * i]; sq += a[2 * i + 1]; }
      sm *= 1.f / D;
      *(LAS f32x2*)(lds + L_ST + ((hw & 1) * 64 + l) * 8) = f32x2{ln_std(fmaxf(sq * (1.f / D) - sm * sm, 0.f)), -rbf(sm)};
    }
    SIG(C_EP3);
    if (l < 14) poll(rp + OFF_FS + 8, cb + (l >> 2) * 64 + (l & 3) * 8 - l, 14, e + 1, l);
#pragma unroll 1
    for (int n = 0; n < 4; ++n) {
      await_<C_G3, 4, 1>(cb_ + n * 4);
#pragma unroll 1
      for (int k = (n ? 7 * n + 1 : 0) + hw; k < 7 * n + 8 && k < 27; k += 4) glds5s(lb + (k < 8 ? k + 23 : k - 8) * 5120, w4p + k * 5120, l16);
    }
    wait_vm<0>();
    SIG(C_W4);
    return;
  }
  __builtin_amdgcn_s_setprio(1);
  const f32x4 z4 = {0, 0, 0, 0};
  u32x4 tb2[4];
  u64 e;
  u32 tag;
  {
    const int m = l & 15, q = l >> 4;
    u32x4 r8 = radix8_const<true>(l, s2);
    tie(r8);
    u32 xrd = L_XR + w * 2560 + q * 320 + m * 4, ywr = L_A + m * K1_TS + (w >> 1) * 512 + q * 64 + (w & 1) * 8;
    u32 xo = OFF_X + ((l >> 5) * 128 + 8 * w + ((l >> 2) & 7)) * 16384 + s2 * 64 + (l & 3) * 16;
    u32 yrd = L_A + 3 * (l & 3) * K1_TS + (l >> 5) * 512 + (8 * w + ((l >> 2) & 7)) * 16, yrd2 = yrd + ((l & 3) == 3 ? 1 : 2) * K1_TS;
    asm volatile("" : "+v"(xrd), "+v"(ywr), "+v"(xo), "+v"(yrd), "+v"(yrd2));
    u32x4 tbl[12];
    wait_vm<4>();
    CBAR(C_TB);
#pragma unroll
    for (int i = 0; i < 12; ++i) tbl[i] = lds4(lds + L_B + l16 + i * 1024);
#pragma unroll
    for (int i = 0; i < 4; ++i) tb2[i] = lds4(lds + L_B + l16 + 12288 + i * 1024);
    const f32x4 ns[2] = {neg4(tbl[10]), neg4(tbl[11])};
    wait_vm<0>();
    u32 xv[2][5];
#pragma unroll
    for (int rr = 0; rr < 2; ++rr)
#pragma unroll
      for (int j = 0; j < 5; ++j) xv[rr][j] = *(const LAS u32*)(lds + xrd + rr * 1280 + j * 64);
    float sm[2] = {0.f, 0.f}, sq[2] = {0.f, 0.f};
#pragma unroll
    for (int j = 0; j < 5; ++j)
#pragma unroll
      for (int rr = 0; rr < 2; ++rr) { sm[rr] = dot2(xv[rr][j], 0x3f803f80u, sm[rr]); sq[rr] = dot2(xv[rr][j], xv[rr][j], sq[rr]); }
    float red = row_sum(fold16(fold32(sm[0], sq[0]), fold32(sm[1], sq[1]))) * (1.f / D);
    auto sw = __builtin_amdgcn_permlane32_swap(__float_as_uint(red), __float_as_uint(red), false, false);
    const float mean = __uint_as_float(sw[0]);
    const float rsv = __builtin_amdgcn_rcpf(ln_std(fmaxf(__uint_as_float(sw[1]) - mean * mean, 0.f))), nbv = -rbf(mean) * rsv;
    u32x4 yo[2][2];
    f32x4 pr[2][2], pi[2][2];
#pragma unroll
    for (int rr = 0; rr < 2; ++rr) {
      const float rs_ = rdl(rsv, 16 * rr), nb = rdl(nbv, 16 * rr);
      f32x2 v[5];
#pragma unroll
      for (int j = 0; j < 5; ++j) v[j] = fma2(up2(xv[rr][j]), sp2(rs_), sp2(nb));
#pragma unroll
      for (int t = 0; t < 2; ++t) {
        const u32x4 a = {pk(v[0][t], v[1][t]), pk(v[2][t], v[3][t]), pk(v[4][t], 0.f), 0u};
        pr[rr][t] = mfma16(a, tbl[0], z4); pi[rr][t] = mfma16(a, tbl[1], z4);
      }
    }
#pragma unroll
    for (int rr = 0; rr < 2; ++rr) {
#pragma unroll
      for (int t = 0; t < 2; ++t) rot4(pr[rr][t], pi[rr][t], asf(tbl[8 + t]), asf(tbl[10 + t]), ns[t], asf(tbl[8 + t]));
      const u32x4 br = pkc(pr[rr][0], pr[rr][1]), bi = pkc(pi[rr][0], pi[rr][1]);
#pragma unroll
      for (int t = 0; t < 2; ++t) {
        pr[rr][t] = mfma16(tbl[4 + t], bi, mfma16(tbl[2 + t], br, z4));
        pi[rr][t] = mfma16(tbl[6 + t], br, mfma16(tbl[2 + t], bi, z4));
      }
    }
#pragma unroll
    for (int rr = 0; rr < 2; ++rr)
#pragma unroll
      for (int t = 0; t < 2; ++t) yo[rr][t] = pkc(pr[rr][t], pi[rr][t]);
    if (m < 11) {
#pragma unroll
      for (int t = 0; t < 2; ++t)
#pragma unroll
        for (int r = 0; r < 4; ++r) *(LAS u32x2*)(lds + ywr + t * 256 + r * 16) = u32x2{yo[0][t][r], yo[1][t][r]};
    }
    e = (ep + 1) << 8;
    tag = ((u32)ep + 1u) ^ 0x5a5a5a5au;
    CBAR(C_P1);
    f32x16 z16;
#pragma unroll
    for (int i = 0; i < 16; ++i) z16[i] = 0.f;
    const u32x4 bb[3] = {lds4(lds + yrd), lds4(lds + yrd + K1_TS), lds4(lds + yrd2)};
    SB();
    f32x16 o[3];
#pragma unroll
    for (int n = 0; n < 3; ++n) o[n] = mfma32(r8, bb[n], z16);
    SB();
#pragma unroll
    for (int i = 0; i < 4; ++i)
      st1(u32x4{pk(o[0][2 * i], o[0][2 * i + 1]), pk(o[1][2 * i], o[1][2 * i + 1]), pk(o[2][2 * i], o[2][2 * i + 1]), tag}, rs, xo, i * 524288);
    SIG(C_ST);
  }
  {
    const int l = opq(l16) >> 4, m = l & 15, q = l >> 4;
    u32 goff = (l >> 2) * 64 + (((l & 3) ^ (l >> 4)) * 16), frd = L_B + (64 * q + m) * 64 + ((w ^ (m >> 2)) * 16);
    asm volatile("" : "+v"(goff), "+v"(frd));
    f32x4 wre[3], nwi[3], nwr[3];
    const f32x4 ntw = neg4(tb2[3]);
    auto gather = [&]() {
      const int par = (xcd & 1) * 2;
      const u32 p = (m + 64 * q) * 64 + ((m >> 1) & 3) * 16;
#pragma unroll
      for (int t = 0; t < 3; ++t) {
        const int k1 = (3 * w + t) < 11 ? 3 * w + t : 10, o = i5 < 16 ? 2 * k1 + par : 28 - 2 * k1 + par;
#pragma unroll
        for (int r = 0; r < 4; ++r) {
          wre[t][r] = bflo(*(const LAS u16*)(lds + L_WF + r * 1024 + (p ^ o)));
          nwi[t][r] = __uint_as_float(((u32) * (const LAS u16*)(lds + L_WF + r * 1024 + (p ^ (o + 32))) << 16) ^ (i5 < 16 ? 0x80000000u : 0u));
        }
        tie(wre[t]); tie(nwi[t]);
        nwr[t] = neg4(__builtin_bit_cast(u32x4, wre[t]));
      }
    };
    const bool got = __builtin_amdgcn_readfirstlane(peek(lds, C_WF)) >= 4;
    asm volatile("" ::: "memory");
    if (got) gather();
    wait_vm<0>();
    u32x4 in[4];
    load_tagged(in, rs, goff, OFF_X + s2 * 16384 + w * 4096, 1024, tag);
#pragma unroll
    for (int i = 0; i < 4; ++i) *(LAS u32x4*)(lds + L_B + w * 4096 + i * 1024 + l16) = in[i];
    SIG(C_P2A);
    if (!got) { AWAIT(C_WF, 4); gather(); }
    AWAIT(C_P2A, 4);
#pragma unroll
    for (int i = 0; i < 4; ++i) in[i] = lds4(lds + frd + i * 1024);
    const u32x4 fre = tb2[0], fim = tb2[1];
    const f32x4 twc = __builtin_bit_cast(f32x4, tb2[2]), tws = __builtin_bit_cast(f32x4, tb2[3]);
    u32x4 a[3];
    f32x4 pr[3], pi[3];
#pragma unroll
    for (int t = 0; t < 3; ++t) {
      a[t] = u32x4{in[0][t], in[1][t], in[2][t], in[3][t]};
      pr[t] = mfma16(a[t], fre, z4); pi[t] = mfma16(a[t], fim, z4);
    }
#pragma unroll
    for (int t = 0; t < 3; ++t) {
      rot4(pr[t], pi[t], twc, tws, ntw, twc);
      a[t] = pkc(pr[t], pi[t]);
      pr[t] = mfma16(fre, a[t], z4); pi[t] = mfma16(fim, a[t], z4);
    }
#pragma unroll
    for (int t = 0; t < 3; ++t) {
      rot4(pr[t], pi[t], wre[t], nwi[t], nwi[t], nwr[t]);
      a[t] = pkc(pr[t], pi[t]);
      pr[t] = mfma16(a[t], fre, z4); pi[t] = mfma16(a[t], fim, z4);
    }
#pragma unroll
    for (int t = 0; t < 3; ++t) {
      rot4(pr[t], pi[t], twc, tws, ntw, twc);
      a[t] = pkc(pr[t], pi[t]);
      pr[t] = mfma16(fre, a[t], z4); pi[t] = mfma16(fim, a[t], z4);
    }
#pragma unroll
    for (int i = 0; i < 4; ++i) *(LAS u32x4*)(lds + frd + i * 1024) = u32x4{pk(pr[0][i], pi[0][i]), pk(pr[1][i], pi[1][i]), pk(pr[2][i], pi[2][i]), tag ^ 0x80000000u};
    CBAR(C_P2B);
#pragma unroll
    for (int i = 0; i < 4; ++i) in[i] = lds4(lds + L_B + w * 4096 + i * 1024 + l16);
#pragma unroll
    for (int i = 0; i < 4; ++i) st1(in[i], rs, goff, OFF_T + s2 * 16384 + w * 4096 + i * 1024);
  }
  const int T = tb * 4 + w;
  const int kbp = g * 5 + ((cb & 1) ? 3 : 0);
  f32x4 res[2][5];
  u32 av0, av1, ah0, ah1, so[8], pk_;
  const int T3 = tb3 * 8 + 2 * w;
  auto loadp = [&](u32 soff, int imm, int j, bool split) {
    if (!split) return ld1(rs, (j ? av1 : av0) + imm, soff);
    const u32x2 a = ld1h(rs, (j ? ah1 : ah0) + imm, soff), c = ld1h(rs, (j ? ah1 : ah0) + imm + 512, soff);
    return u32x4{a[0], a[1], c[0], c[1]};
  };
  auto load = [&](int k, int j) { return loadp(so[k / 5 * 2 + (k % 5 == 4)], k % 5 % 4 * 1024, j, k % 5 == 2); };
  auto set_rot = [&](int r) {
#pragma unroll
    for (int a = 0; a < 4; ++a) { so[2 * a] = wrapk(5 * a + r, 20) * 1024; so[2 * a + 1] = so[2 * a] + 4096; asm volatile("" : "+s"(so[2 * a]), "+s"(so[2 * a + 1])); }
  };
  auto half_off = [&](u32 a) { return (a & ~1023u) | ((a & 1023u) >> 1); };
  auto trl = [&](int pitch) { const u32 l = opq(l16) >> 4; return ((l >> 4) * 4 + ((l >> 2) & 3)) * pitch + (l & 3) * 8; };
  {
    const int l = opq(l16) >> 4, m = l & 15, q = l >> 4;
    u32x4 tbl[16];
#pragma unroll
    for (int i = 0; i < 16; ++i) tbl[i] = lds4(lds + L_T3 + l16 + i * 1024);
    u32x4 r8 = radix8_const<false>(l, s2);
    tie(r8);
    f32x4 y[2][2][2];
    {
      const u32 xrd0 = L_XR + 2 * w * 1280 + m * 64 + q * 16;
#pragma unroll
      for (int rr = 0; rr < 2; ++rr) {
        const u32x4 x0 = lds4(lds + xrd0 + rr * 1280), x1 = lds4(lds + (m < 4 ? xrd0 + rr * 1280 + 1024 : L_ZERO));
#pragma unroll
        for (int t = 0; t < 2; ++t) { y[rr][0][t] = mfma16(tbl[14 + t], x0, z4); y[rr][1][t] = mfma16(tbl[14 + t], x1, z4); tie(y[rr][0][t]); tie(y[rr][1][t]); }
      }
    }
#pragma unroll
    for (int i = 0; i < 14; ++i) tie(tbl[i]);
    const f32x4 ns[2] = {neg4(tbl[12]), neg4(tbl[13])};
    u32 zo = OFF_T + ((l >> 5) * 128 + 8 * w + ((l >> 2) & 7)) * 16384 + s2 * 64 + (l & 3) * 16;
    u32 zrd = L_WF + 2 * w * K3_RS + (m < 11 ? m : 10) * K3_KS + q * 16;
    u32 hwr = L_A + m * 528 + q * 128 + w * 32, hws = L_A + m * 528 + q * 64 + w * 16;
    u32 hrd = L_A + (l >> 5) * 528 + (l & 31) * 16, hgo[3];
#pragma unroll
    for (int n = 0; n < 3; ++n) {
      const int n1 = 2 * (w + 4 * n) + (l >> 5);
      hgo[n] = n1 * 1024 + (n1 % 5 == 2 ? ((l >> 2) & 7) * 128 + (s2 & 1) * 64 + (l & 3) * 16 : ((l >> 3) & 3) * 256 + (s2 & 1) * 128 + (l & 7) * 16);
    }
    u32 zwr = L_WF + 4 * (l >> 5) * K3_RS + 3 * (l & 3) * K3_KS + (8 * w + ((l >> 2) & 7)) * 4;
    asm volatile("" : "+v"(zrd), "+v"(zo), "+v"(hwr), "+v"(hrd), "+v"(hws), "+v"(hgo[0]), "+v"(hgo[1]), "+v"(hgo[2]), "+v"(zwr));
    {
      const int shift = (g & 1) ? -(1 + (g >> 1)) : (1 + (g >> 1));
      auto src = [&](int Tt) {
        const int rho = Tt * 16 + m, sp = (rho & 7) * 256 + (rho >> 3) - shift, rq = (sp & 255) * 8 + (sp >> 8);
        return (sp >= 0 && sp < S) ? OFF_H + (rq >> 4) * 20480 + q * 256 + (rq & 15) * 16 : 0x80000000u;
      };
      av0 = src(T3); av1 = src(T3 + 1);
      asm volatile("" : "+v"(av0), "+v"(av1));
      set_rot(r5);
    }
    wait_vm<0>();
    __builtin_amdgcn_s_sleep(8);
    u32x4 zb[4];
    load_tagged(zb, rs, zo, 0, 524288, tag ^ 0x80000000u);
    f32x16 z16;
#pragma unroll
    for (int i = 0; i < 16; ++i) z16[i] = 0.f;
    f32x16 o[3];
#pragma unroll
    for (int n = 0; n < 3; ++n) o[n] = mfma32(r8, u32x4{zb[0][n], zb[1][n], zb[2][n], zb[3][n]}, z16);
    SB();
#pragma unroll
    for (int n = 0; n < 3; ++n)
#pragma unroll
      for (int i = 0; i < 4; ++i) *(LAS u32*)(lds + zwr + n * K3_KS + i * K3_RS) = pk(o[n][2 * i], o[n][2 * i + 1]);
    CBAR(C_B1);
    u32x4 za[2][2];
#pragma unroll
    for (int rr = 0; rr < 2; ++rr) { za[rr][0] = lds4(lds + zrd + rr * K3_RS); za[rr][1] = lds4(lds + zrd + rr * K3_RS + 64); }
    SB();
    f32x4 qr[2][2], qi[2][2];
#pragma unroll
    for (int t = 0; t < 2; ++t)
#pragma unroll
      for (int rr = 0; rr < 2; ++rr) {
        qr[rr][t] = mfma16(za[rr][1], tbl[2 * t + 1], mfma16(za[rr][0], tbl[2 * t], z4));
        qi[rr][t] = mfma16(za[rr][1], tbl[2 * t + 5], mfma16(za[rr][0], tbl[2 * t + 4], z4));
      }
#pragma unroll
    for (int t = 0; t < 2; ++t)
#pragma unroll
      for (int rr = 0; rr < 2; ++rr) {
        rot4(qr[rr][t], qi[rr][t], asf(tbl[10 + t]), asf(tbl[12 + t]), ns[t], asf(tbl[10 + t]));
        const u32x4 a = pkc(qr[rr][t], qi[rr][t]);
        y[rr][0][t] = mfma16(a, tbl[8], y[rr][0][t]); y[rr][1][t] = mfma16(a, tbl[9], y[rr][1][t]);
      }
    u32x4 hp[2][2];
#pragma unroll
    for (int rr = 0; rr < 2; ++rr)
#pragma unroll
      for (int n = 0; n < 2; ++n) hp[rr][n] = u32x4{pk2(lo2(y[rr][n][0])), pk2(hi2(y[rr][n][0])), pk2(lo2(y[rr][n][1])), pk2(hi2(y[rr][n][1]))};
#pragma unroll
    for (int n = 0; n < 2; ++n)
      if (n == 0 || m < 4) {
        const bool sp = n ? m == 1 : (m == 2 || m == 7 || m == 12);
        const u32x4 a = hp[0][n], c = hp[1][n];
        *(LAS u32x4*)(lds + (sp ? hws : hwr) + n * 16 * 528) = sp ? u32x4{a[0], a[1], c[0], c[1]} : a;
        *(LAS u32x4*)(lds + (sp ? hws + 256 : hwr + 16) + n * 16 * 528) = sp ? u32x4{a[2], a[3], c[2], c[3]} : c;
      }
    CBAR(C_B2);
    if (b == 0 && tid == 0) __hip_atomic_store((u64*)(ws + OFF_EP), ep + 1, __ATOMIC_RELAXED, __HIP_MEMORY_SCOPE_AGENT);
    const u32 hb = OFF_H + (s2 >> 1) * 20480;
#pragma unroll
    for (int n = 0; n < 3; ++n)
      if (n < 2 || w < 2) st1(lds4(lds + hrd + (w + 4 * n) * 1056), rs, hgo[n], hb);
    SB();
    float sm[2] = {0.f, 0.f}, sq[2] = {0.f, 0.f};
#pragma unroll
    for (int j = 0; j < 8; ++j)
#pragma unroll
      for (int rr = 0; rr < 2; ++rr) { const u32 hv = hp[rr][j >> 2][j & 3]; sm[rr] = dot2(hv, 0x3f803f80u, sm[rr]); sq[rr] = dot2(hv, hv, sq[rr]); }
    float red = row_sum(fold16(fold32(sm[0], sq[0]), fold32(sm[1], sq[1]))) * (1.f / D);
    asm volatile("" : "+v"(red));
    arrive<C_F3>(cb_, fl + OFF_FM, 4, e + 3, l);
    auto sw = __builtin_amdgcn_permlane32_swap(__float_as_uint(red), __float_as_uint(red), false, false);
    const float mean = __uint_as_float(sw[0]), var = fmaxf(__uint_as_float(sw[1]) - mean * mean, 0.f);
    const float mb1 = rbf(mean), sd1 = ln_std(var), rs1 = __builtin_amdgcn_rcpf(sd1);
    const float mb2 = rbf((mean - mb1) * rs1), sd2 = ln_std(var * rs1 * rs1);
    f32x4 o4 = {sd1 * sd2, -(mb1 + mb2 * sd1), mb1, rs1};
    if (l == 0 || l == 16) *(LAS f32x4*)(lds + L_XR + w * 32 + l) = o4;
    SIG(C_FS);
  }
  {
    const int l = opq(l16) >> 4, m = l & 15, q = l >> 4;
    u32 tpo = T3 * 20480 + kbp * 1024 + l16;
    u32 tso = T3 * 20480 + (g * 5 + 2) * 1024 + (cb & 1) * 512 + l * 8;
    u32 p0 = trl(160);
#pragma unroll
    for (int i = 0; i < 5; ++i) { res[0][i] = z4; res[1][i] = z4; }
    if (!tA) {
      if (w == 1) {
        AWAIT(C_FS, 4);
        if (l < 8) st1(lds4(lds + L_XR + l16), rs, l16, OFF_S1 + s2 * 128);
        wait_vm<0>();
        if (l < 8) __hip_atomic_store((u64*)(fl + OFF_FS + l * (256 * FSTR)), e + 1, __ATOMIC_RELAXED, __HIP_MEMORY_SCOPE_AGENT);
      }
      if (w == 0) { poll(rp + OFF_FM, 16 * tb3, 16, e + 3, l); poll(rp + OFF_FS, 16 * tb3, 16, e + 1, l); SIG(C_R2); } else AWAIT(C_R2, 1);
#pragma unroll
      for (int j = 0; j < 2; ++j) {
        const u32x4 h0 = ld1(rs, tpo, OFF_H + j * 20480), h1 = ld1(rs, tpo, OFF_H + j * 20480 + 1024);
        const u32x2 h2 = ld1h(rs, tso, OFF_H + j * 20480), mr = ld1h(rs, m * 16 + 8, OFF_S1 + (T3 + j) * 256);
        const u32 hk[10] = {h0[0], h0[1], h0[2], h0[3], h1[0], h1[1], h1[2], h1[3], h2[0], h2[1]};
        f32x2 xb[10];
#pragma unroll
        for (int i = 0; i < 10; ++i) xb[i] = up2(pk2(up2(pk2(up2(hk[i]) - sp2(__uint_as_float(mr[0])))) * sp2(__uint_as_float(mr[1]))));
#pragma unroll
        for (int i = 0; i < 5; ++i) res[j][i] = cat4(xb[2 * i], xb[2 * i + 1]);
      }
      av0 = OFF_G1 + T3 * 20480 + l16; av1 = av0 + 20480;
    }
    ah0 = half_off(av0); ah1 = half_off(av1);
    asm volatile("" : "+v"(av0), "+v"(av1), "+v"(ah0), "+v"(ah1));
#pragma unroll
    for (int i = 0; i < 5; ++i) { tie(res[0][i]); tie(res[1][i]); }
    AWAIT(C_W1, 4);
    if (w == 0) { poll(rp + OFF_FM, tA ? pfirst : lidA, tA ? pn : 8, tA ? e + 3 : e + 4, l); SIG(C_R1); } else AWAIT(C_R1, 1);
    f32x4 cs[5];
    f32x2 sc[2];
    gemm32<20, 5, 4, 2>(res, lds, p0, [](int k) { return k * 5120; }, load, [&](auto KI) {
      if constexpr (KI.value == 17) pk_ = peek(lds, C_EP1);
      if constexpr (KI.value == 18) {
        if (tA) CHECK(pk_, C_EP1, 4);
#pragma unroll
        for (int i = 0; i < 5; ++i) cs[i] = ldsf(lds + L_MISC + q * 16 + i * 64);
#pragma unroll
        for (int j = 0; j < 2; ++j) sc[j] = *(const LAS f32x2*)(lds + L_ST + (w * 32 + j * 16 + m) * 8);
      }
    });
    SIG(C_E1);
    if (tA) {
#pragma unroll
      for (int j = 0; j < 2; ++j) {
        const Gelu gc = gelu_consts(sc[j][0], sc[j][1]);
#pragma unroll
        for (int pp = 0; pp < 2; ++pp)
          st1(pk44(gelu4(res[j][2 * pp], cs[2 * pp], gc), gelu4(res[j][2 * pp + 1], cs[2 * pp + 1], gc)), rs, tpo, OFF_G1 + j * 20480 + pp * 1024);
        const f32x4 g4 = gelu4(res[j][4], cs[4], gc);
        st1h(u32x2{pk2(lo2(g4)), pk2(hi2(g4))}, rs, tso, OFF_G1 + j * 20480);
      }
    } else {
      float rd[2];
#pragma unroll
      for (int j = 0; j < 2; ++j) {
        const u32x4 x0 = pk44(res[j][0], res[j][1]), x1 = pk44(res[j][2], res[j][3]);
        const u32x2 x2 = {pk2(lo2(res[j][4])), pk2(hi2(res[j][4]))};
        st1(x0, rs, tpo, OFF_X1 + j * 20480);
        st1(x1, rs, tpo, OFF_X1 + j * 20480 + 1024);
        st1h(x2, rs, tso, OFF_X1 + j * 20480);
        const u32 xk[10] = {x0[0], x0[1], x0[2], x0[3], x1[0], x1[1], x1[2], x1[3], x2[0], x2[1]};
        float sm = 0.f, sq = 0.f;
#pragma unroll
        for (int i = 0; i < 10; ++i) { sm = dot2(xk[i], 0x3f803f80u, sm); sq = dot2(xk[i], xk[i], sq); }
        rd[j] = fold32(sm, sq);
      }
      const float r4 = fold16(rd[0], rd[1]);
      __builtin_amdgcn_raw_buffer_store_b32(__float_as_uint(r4), rs, (q & 1) * 1024 + (q >> 1) * 64 + m * 4, OFF_S2 + T3 * 1024 + cb * 128, 16);
    }
  }
  {
    const int l = opq(l16) >> 4, m = l & 15, q = l >> 4;
    av0 = OFF_X1 + T3 * 20480 + l16; av1 = av0 + 20480;
    u32 p0 = trl(224);
    ah0 = half_off(av0); ah1 = half_off(av1);
    asm volatile("" : "+v"(av0), "+v"(av1), "+v"(ah0), "+v"(ah1), "+v"(p0));
    set_rot(r3);
    arrive<C_F4>(cb_, fl + OFF_FM, 4, e + 4, l);
    AWAIT(C_W2, 4);
    if (w == 0) { poll(rp + OFF_FM, lidA + 32, 8, e + 4, l); SIG(C_R3); } else AWAIT(C_R3, 1);
    f32x4 acc[2][7] = {{z4, z4, z4, z4, z4, z4, z4}, {z4, z4, z4, z4, z4, z4, z4}}, cs[7];
    f32x2 sc[2];
    u32x4 x0, x1, h0, h1;
    u32x2 x2, h2;
    gemm32<20, 7, 6, 3>(acc, lds, p0, [](int k) { return (k + 16) % 20 * 7168; }, load, [&](auto KI) {
      constexpr int k = KI.value;
      if constexpr (k == 1) pk_ = peek(lds, C_W3);
      if constexpr (k == 2) CHECK(pk_, C_W3, 4);
      if constexpr (k == 3 || k == 8 || k == 13 || k == 18) SIG(C_G3 + k / 5);
      if constexpr (k == 15) {
        const u32 tpo = T * 20480 + kbp * 1024 + l16;
        x0 = ld1(rs, tpo, OFF_X1); x1 = ld1(rs, tpo, OFF_X1 + 1024); h0 = ld1(rs, tpo, OFF_H);  h1 = ld1(rs, tpo, OFF_H + 1024);
      }
      if constexpr (k == 16) {
        const u32 tso = T * 20480 + (g * 5 + 2) * 1024 + (cb & 1) * 512 + l * 8;
        x2 = ld1h(rs, tso, OFF_X1); h2 = ld1h(rs, tso, OFF_H);
      }
      if constexpr (k == 17) pk_ = peek(lds, C_EP3);
      if constexpr (k == 18) {
        CHECK(pk_, C_EP3, 4);
#pragma unroll
        for (int i = 0; i < 7; ++i) cs[i] = ldsf(lds + L_MISC + 320 + q * 16 + i * 64);
#pragma unroll
        for (int j = 0; j < 2; ++j) sc[j] = *(const LAS f32x2*)(lds + L_ST + (w * 32 + j * 16 + m) * 8);
      }
    });
    const Gelu gc[2] = {gelu_consts(sc[0][0], sc[0][1]), gelu_consts(sc[1][0], sc[1][1])};
    const int kb0 = (cb3 >> 1) * 7;
#pragma unroll
    for (int j = 0; j < 2; ++j) {
      const u32 gb = OFF_G3 + (T3 + j) * 55296;
#pragma unroll
      for (int pp = 0; pp < 3; ++pp)
        if (pp == 0 || cb3 < 15)
          st1(pk44(gelu4(acc[j][2 * pp], cs[2 * pp], gc[j]), gelu4(acc[j][2 * pp + 1], cs[2 * pp + 1], gc[j])), rs, l16, gb + g3pos(kb0 + (cb3 & 1) * 4 + pp) * 1024);
      const f32x4 g6 = gelu4(acc[j][6], cs[6], gc[j]);
      st1h(u32x2{pk2(lo2(g6)), pk2(hi2(g6))}, rs, l * 8, gb + g3pos(kb0 + 3) * 1024 + (cb3 & 1) * 512);
    }
    const u32 xk[10] = {x0[0], x0[1], x0[2], x0[3], x1[0], x1[1], x1[2], x1[3], x2[0], x2[1]};
    const u32 hh[10] = {h0[0], h0[1], h0[2], h0[3], h1[0], h1[1], h1[2], h1[3], h2[0], h2[1]};
#pragma unroll
    for (int i = 0; i < 5; ++i) { res[0][i] = cat4(up2(xk[2 * i]) + up2(hh[2 * i]), up2(xk[2 * i + 1]) + up2(hh[2 * i + 1])); tie(res[0][i]); }
  }
  {
    av0 = OFF_G3 + T * 55296 + (tb & 1) * 27648 + l16; av1 = OFF_G3 + (T ^ 4) * 55296 + (tb & 1) * 27648 + l16;
    u32 p0 = trl(160);
    ah0 = half_off(av0); ah1 = half_off(av1);
    asm volatile("" : "+v"(av0), "+v"(av1), "+v"(ah0), "+v"(ah1), "+v"(p0));
#pragma unroll
    for (int k = 0; k < 7; ++k) { so[k] = k * 4096; asm volatile("" : "+s"(so[k])); }
#pragma unroll
    for (int i = 0; i < 5; ++i) res[1][i] = z4;
    arrive<C_F6>(cb_, fl + OFF_FM, 4, e + 6, l16 >> 4);
    AWAIT(C_W4, 4);
    if (w == 0) { poll(rp + OFF_FM, lidA + (l16 >> 7) * 24, 16, e + 6, l16 >> 4); SIG(C_R4); } else AWAIT(C_R4, 1);
    gemm32<27, 5, 6, 3>(res, lds, p0, [](int k) { return (k + 23) % 31 * 5120; }, [&](int k, int j) { return loadp(so[k >> 2], (k & 3) * 1024, j, k % 7 == 3); }, [&](auto KI) {
    });
    {
      const u32 tg = tag ^ 0x40000000u, xb = tb3 * 163840 + cb * 16384 + w * 4096;
      u32 d[12];
#pragma unroll
      for (int i = 0; i < 5; ++i) { d[2 * i] = pk2(lo2(res[1][i])); d[2 * i + 1] = pk2(hi2(res[1][i])); }
      d[10] = d[11] = 0u;
#pragma unroll
      for (int i = 0; i < 4; ++i) st1(u32x4{d[3 * i], d[3 * i + 1], d[3 * i + 2], tg}, rs, l16, ((tb & 1) ? OFF_G1 : OFF_X1) + xb + i * 1024);
      wait_vm<0>();
      u32x4 in[4];
      load_tagged(in, rs, l16, ((tb & 1) ? OFF_X1 : OFF_G1) + xb, 1024, tg);
#pragma unroll
      for (int i = 0; i < 5; ++i) res[0][i] += cat4(up2(in[(2 * i) / 3][(2 * i) % 3]), up2(in[(2 * i + 1) / 3][(2 * i + 1) % 3]));
    }
    const int l = opq(l16) >> 4;
    const int rho2 = T * 16 + (l >> 2);
    const rsrc_t ro = __builtin_amdgcn_make_buffer_rsrc(out, 0, ACT, 0x00020000);
    const u32 op = ((rho2 & 7) * 256 + (rho2 >> 3)) * (D * 2);
    const int srcp = ((((l >> 1) & 1) | ((l & 1) << 1)) * 16 + (l >> 2)) * 4, srcs = ((l & 3) * 16 + (l >> 2)) * 4;
#pragma unroll
    for (int pp = 0; pp < 2; ++pp) {
      const u32x4 d = pk44(res[0][2 * pp], res[0][2 * pp + 1]);
      auto s0 = __builtin_amdgcn_permlane16_swap(d[0], d[2], false, false);
      auto s1 = __builtin_amdgcn_permlane16_swap(d[1], d[3], false, false);
      u32x4 o = {s0[0], s1[0], s0[1], s1[1]};
#pragma unroll
      for (int r = 0; r < 4; ++r) o[r] = __builtin_amdgcn_ds_bpermute(srcp, o[r]);
      __builtin_amdgcn_raw_buffer_store_b128(o, ro, op + (kbp + pp) * 64 + (l & 3) * 16, 0, 0);
    }
    u32x2 so2 = {(u32)__builtin_amdgcn_ds_bpermute(srcs, pk2(lo2(res[0][4]))), (u32)__builtin_amdgcn_ds_bpermute(srcs, pk2(hi2(res[0][4])))};
    __builtin_amdgcn_raw_buffer_store_b64(so2, ro, op + (g * 5 + 2) * 64 + (cb & 1) * 32 + (l & 3) * 8, 0, 0);
  }
}
}

extern "C" void kernel_launch(void* const* d_in, const int* in_sizes, int n_in, void* d_out, int out_size, void* d_ws, size_t ws_size,
                              hipStream_t stream) {
  (void)hipFuncSetAttribute((const void*)fused_block, hipFuncAttributeMaxDynamicSharedMemorySize, L_TOT);
  fused_block<<<256, 512, L_TOT, stream>>>((const u16*)d_in[0], (const u16*)d_in[1], (const u16*)d_in[2], (const u16*)d_in[4], (const u16*)d_in[5],
                                           (const u16*)d_in[6], (const u16*)d_in[7], (char*)d_ws, (u16*)d_out);
}
```
